```python
import torch

def _softmax(x, axis=-1):
    return torch.softmax(x, dim=axis)
def _gelu(x):
    return torch.nn.functional.gelu(x, approximate="tanh")
def _silu(x):
    return torch.nn.functional.silu(x)
def _sigmoid(x):
    return torch.sigmoid(x)
def _layernorm(x, axis=-1, eps=1e-5):
    m = x.mean(dim=axis, keepdim=True)
    v = x.var(dim=axis, keepdim=True, unbiased=False)
    return (x - m) / torch.sqrt(v + eps)
def _scatter_add(tgt, idx, src, axis=0):
    return tgt.clone().index_add_(axis, idx, src)
def _segment_sum(src, seg, n):
    out = torch.zeros((n,) + src.shape[1:], dtype=src.dtype)
    return out.index_add_(0, seg, src)
def _pad(x, pw):
    flat = tuple(v for pair in reversed(pw) for v in pair)
    return torch.nn.functional.pad(x, flat)

kernel_name = 'gather_scatter__kernel_systems_refs_featgraph__graph'

def setup_inputs():

    _w = (lambda a: (lambda t: t.to(torch.bfloat16) if t.is_floating_point() else t)(torch.as_tensor(a)))
    N, deg, D = 10000, 32, 128
    E = N * deg
    scale = D ** -0.5
    src = (lambda lo, hi, *s: torch.randint(lo, hi, s))(0, N, E).to(torch.int64)
    dst = (lambda lo, hi, *s: torch.randint(lo, hi, s))(0, N, E).to(torch.int64)
    return {
        "x": _w((lambda *s: torch.randn(*s, dtype=torch.bfloat16) * 0.8)(N, D)),
        "src": _w(src),
        "dst": _w(dst),
        "w_q": _w((lambda *s: torch.randn(*s, dtype=torch.bfloat16) * 0.8)(D, D) * scale),
        "w_v": _w((lambda *s: torch.randn(*s, dtype=torch.bfloat16) * 0.05)(D, D) * scale),
    }

def reference(x, src, dst, w_q, w_v):
    N, D = x.shape
    xs = x[src]
    xd = x[dst]
    q = xd @ w_q
    logits = torch.einsum('ed,ed->e', xs, q) * (float(D) ** -0.5)
    alpha = _sigmoid(logits)
    msg = alpha[:, None] * (xs @ w_v)
    return _segment_sum(msg, dst, N)
```

```cpp
#include <hip/hip_runtime.h>
#include <hip/hip_bf16.h>
#include <stdint.h>

__device__ inline float bf2f(uint16_t v) { return __uint_as_float(((uint32_t)v) << 16); }
__device__ inline uint16_t f2bf(float f) { __hip_bfloat16 b = __float2bfloat16(f); return *(uint16_t*)&b; }

__global__ void k_q(const uint16_t* x, const uint16_t* wq, float* Q, float* Y, int N) {
  int n = blockIdx.x; int c = threadIdx.x;
  float acc = 0.f;
  for (int k = 0; k < 128; ++k) acc += bf2f(x[n * 128 + k]) * bf2f(wq[k * 128 + c]);
  Q[n * 128 + c] = bf2f(f2bf(acc));
  Y[n * 128 + c] = 0.f;
}
__global__ void k_e(const uint16_t* x, const int* src, const int* dst, const float* Q, float* Y, int E) {
  int e = blockIdx.x * 4 + (threadIdx.x >> 6); int l = threadIdx.x & 63;
  if (e >= E) return;
  int s = src[e], d = dst[e];
  float a = bf2f(x[s * 128 + l]), b = bf2f(x[s * 128 + 64 + l]);
  float p = a * Q[d * 128 + l] + b * Q[d * 128 + 64 + l];
  for (int o = 32; o; o >>= 1) p += __shfl_xor(p, o);
  float lg = p * 0.08838834764831845f;
  float al = 1.f / (1.f + __expf(-lg));
  atomicAdd(&Y[d * 128 + l], al * a);
  atomicAdd(&Y[d * 128 + 64 + l], al * b);
}
__global__ void k_o(const float* Y, const uint16_t* wv, uint16_t* out) {
  int n = blockIdx.x; int c = threadIdx.x;
  float acc = 0.f;
  for (int k = 0; k < 128; ++k) acc += Y[n * 128 + k] * bf2f(wv[k * 128 + c]);
  out[n * 128 + c] = f2bf(acc);
}
extern "C" void kernel_launch(void* const* d_in, const int* in_sizes, int n_in, void* d_out, int out_size, void* d_ws, size_t ws_size, hipStream_t stream) {
  const uint16_t* x = (const uint16_t*)d_in[0];
  const int* src = (const int*)d_in[1];
  const int* dst = (const int*)d_in[2];
  const uint16_t* wq = (const uint16_t*)d_in[3];
  const uint16_t* wv = (const uint16_t*)d_in[4];
  int N = in_sizes[0] / 128, E = in_sizes[1];
  float* Q = (float*)d_ws; float* Y = Q + (size_t)N * 128;
  k_q<<<N, 128, 0, stream>>>(x, wq, Q, Y, N);
  k_e<<<(E + 3) / 4, 256, 0, stream>>>(x, src, dst, Q, Y, E);
  k_o<<<N, 128, 0, stream>>>(Y, wv, (uint16_t*)d_out);
}
```

```cpp
#include <hip/hip_runtime.h>
#include <hip/hip_bf16.h>
#include <stdint.h>

typedef unsigned u32;
typedef u32 u32x2 __attribute__((ext_vector_type(2)));
typedef u32 u32x4 __attribute__((ext_vector_type(4)));
typedef float f32x2 __attribute__((ext_vector_type(2)));
typedef float f32x4 __attribute__((ext_vector_type(4)));
typedef short s16x4 __attribute__((ext_vector_type(4)));
typedef __bf16 bf16x2 __attribute__((ext_vector_type(2)));
typedef __bf16 bf16x8 __attribute__((ext_vector_type(8)));
typedef __amdgpu_buffer_rsrc_t rsrc_t;
#define LAS __attribute__((address_space(3)))

#define DIAG 0
#define STAMP(i) do {} while (0)
#define FLUSH() do {} while (0)

constexpr int NB = 256;
constexpr int NP = 128;
constexpr int MAXN = 48;
constexpr int CAP = 64;
constexpr int LSTR = 68;
constexpr int RSTR = 272;
constexpr u32 EMPTY = 0xFFFFFu, OVF = 0xFFFFEu;
constexpr u32 OOB = 0x80000000u;

constexpr int L_WQ = 0;
constexpr int L_WV = 32768;
constexpr int L_STAGE = 65536;
constexpr int L_XO = 98304;
constexpr int L_Q = L_XO + MAXN * 256;
constexpr int L_Y = L_Q + MAXN * RSTR;
constexpr int L_LIST = L_Y + MAXN * RSTR;
constexpr int L_CNT = L_LIST + MAXN * LSTR * 4;
constexpr int L_PCNT = L_CNT + 256;
constexpr int L_END = L_PCNT + 1024;

constexpr size_t WS_NONCE = 0;
constexpr size_t WS_CELLS = 4096;
constexpr size_t WS_END = WS_CELLS + (size_t)NB * NP * 128;

__device__ __forceinline__ u32 rfl(u32 v) { return __builtin_amdgcn_readfirstlane(v); }
__device__ __forceinline__ rsrc_t mk(const void* p, u32 bytes) { return __builtin_amdgcn_make_buffer_rsrc((void*)p, (short)0, (int)bytes, 0x00020000); }
__device__ __forceinline__ void dma16(rsrc_t r, u32 voff, u32 lds) {
  u32 keep;
  asm volatile("s_mov_b32 %0, m0\n\ts_mov_b32 m0, %3\n\ts_nop 0\n\tbuffer_load_dwordx4 %1, %2, 0 offen lds\n\ts_mov_b32 m0, %0"
               : "=&s"(keep) : "v"(voff), "s"(r), "s"(lds) : "memory");
}
__device__ __forceinline__ void dma4(rsrc_t r, u32 voff, u32 lds) {
  u32 keep;
  asm volatile("s_mov_b32 %0, m0\n\ts_mov_b32 m0, %3\n\ts_nop 0\n\tbuffer_load_dword %1, %2, 0 offen lds\n\ts_mov_b32 m0, %0"
               : "=&s"(keep) : "v"(voff), "s"(r), "s"(lds) : "memory");
}
__device__ __forceinline__ u32x4 ld16_async(rsrc_t r, u32 voff) {
  u32x4 v;
  asm volatile("buffer_load_dwordx4 %0, %1, %2, 0 offen" : "=v"(v) : "v"(voff), "s"(r) : "memory");
  return v;
}
__device__ __forceinline__ void barrier_lds() {
  asm volatile("s_waitcnt lgkmcnt(0)\n\ts_barrier" ::: "memory");
}
__device__ __forceinline__ u32 pk_bf16(float a, float b) {
  f32x2 f = {a, b};
  return __builtin_bit_cast(u32, __builtin_convertvector(f, bf16x2));
}
template <int CTRL> __device__ __forceinline__ float dppf(float v) {
  return __builtin_bit_cast(float, __builtin_amdgcn_update_dpp(0, __builtin_bit_cast(int, v), CTRL, 0xF, 0xF, true));
}
template <int CTRL> __device__ __forceinline__ u32 dppu(u32 v) {
  return (u32)__builtin_amdgcn_update_dpp(0, (int)v, CTRL, 0xF, 0xF, true);
}
__device__ __forceinline__ u32x2 trread(u32 a) {
  return __builtin_bit_cast(u32x2, __builtin_amdgcn_ds_read_tr16_b64_v4i16((LAS s16x4*)a));
}
__device__ __forceinline__ f32x4 mfma16(u32x4 a, u32x4 b, f32x4 c) {
  return __builtin_amdgcn_mfma_f32_16x16x32_bf16(__builtin_bit_cast(bf16x8, a), __builtin_bit_cast(bf16x8, b), c, 0, 0, 0);
}
template <int T> __device__ __forceinline__ f32x4 mfma4(u32 a0, u32 a1, u32x2 b, f32x4 c) {
  u32x2 a = {a0, a1};
  return __builtin_amdgcn_mfma_f32_4x4x4bf16_1k(__builtin_bit_cast(s16x4, a), __builtin_bit_cast(s16x4, b), c, 0, 0, 4 + T);
}
#define LDS32(a) (*(LAS u32*)(a))
#define LDS64(a) (*(LAS u32x2*)(a))
#define LDS128(a) (*(LAS u32x4*)(a))

__global__ __launch_bounds__(1024) void fg_fused(const uint16_t* __restrict__ x, const int* __restrict__ src, const int* __restrict__ dst,
                                                 const uint16_t* __restrict__ wq, const uint16_t* __restrict__ wv, uint16_t* __restrict__ out,
                                                 char* __restrict__ ws, int N, int E
                                                 ) {
  __shared__ __attribute__((aligned(16))) char smem[L_END];
  const u32 lb = (u32)(size_t)(LAS char*)smem;
  const u32 tid = threadIdx.x, lane = tid & 63, w = rfl(tid >> 6), b = blockIdx.x;
  const bool prod = b < NP;
  STAMP(0);
  const rsrc_t rx = mk(x, N * 256);
  const rsrc_t rcell = mk(ws + WS_CELLS, NB * NP * 128);
  const u32 epp = ((E >> 2) + NP - 1) / NP;

  u32x4 d4, s4, n0, n1;
  const u32 grp = b * epp + tid;
  const bool has = tid < epp && grp < (u32)(E >> 2);
  if (prod) {
    const u32 eo = has ? grp * 16 : OOB;
    d4 = ld16_async(mk(dst, E * 4), eo);
    s4 = ld16_async(mk(src, E * 4), eo);
    const rsrc_t rn = mk(ws + WS_NONCE, NB * 16);
    n0 = ld16_async(rn, (tid >> 3) * 16);
    n1 = ld16_async(rn, (tid >> 3) * 16 + 2048);
  }

  if (tid < MAXN * LSTR / 4) LDS128(lb + L_LIST + tid * 16) = u32x4{OOB, OOB, OOB, OOB};
  if (tid < 80) LDS128(lb + L_CNT + tid * 16) = u32x4{0, 0, 0, 0};
  if (prod) {
    LDS128(lb + L_STAGE + tid * 16) = u32x4{EMPTY, EMPTY, EMPTY, EMPTY};
    LDS128(lb + L_STAGE + 16384 + tid * 16) = u32x4{EMPTY, EMPTY, EMPTY, EMPTY};
  }
  barrier_lds();

  const u32 g = lane >> 4, c = lane & 15;
  const u32 wsw = (c ^ ((g << 2) | (w & 3))) * 16;
  u32x4 tag = ld16_async(mk(ws + WS_NONCE, NB * 16), b * 16);
  {
    const rsrc_t rq = mk(wq, 32768);
    dma16(rq, (4 * w + g) * 256 + wsw, lb + L_WQ + w * 1024);
    dma16(rq, (4 * w + g + 64) * 256 + wsw, lb + L_WQ + 16384 + w * 1024);
    const u32 r = 4 * w + g;
    dma16(rx, w < MAXN / 4 ? (b + 256 * r) * 256 + ((c ^ r) & 15) * 16 : OOB, w < MAXN / 4 ? lb + L_XO + w * 1024 : lb + L_Y);
  }
  if (prod) {
    asm volatile("s_waitcnt vmcnt(4)" : "+v"(d4), "+v"(s4), "+v"(n0), "+v"(n1) :: "memory");
    STAMP(1);
    if (has) {
      u32 r[4];
#pragma unroll
      for (int k = 0; k < 4; ++k) r[k] = __hip_atomic_fetch_add((LAS u32*)(lb + L_PCNT + (d4[k] & 255) * 4), 1u, __ATOMIC_RELAXED, __HIP_MEMORY_SCOPE_WORKGROUP);
#pragma unroll
      for (int k = 0; k < 4; ++k) {
        const u32 bk = d4[k] & 255, rr = min(r[k], 31u);
        const u32 idx = (((rr & 7) << 2) | (rr >> 3)) ^ (bk & 31);
        LDS32(lb + L_STAGE + bk * 128 + idx * 4) = r[k] >= 31u ? OVF : ((s4[k] << 6) | (d4[k] >> 8));
      }
    }
    barrier_lds();
    u32x4 v0 = LDS128(lb + L_STAGE + tid * 16), v1 = LDS128(lb + L_STAGE + 16384 + tid * 16);
    v0 |= (n0 + 0x100000u) & 0xFFF00000u;
    v1 |= (n1 + 0x100000u) & 0xFFF00000u;
    const u32 co = ((tid >> 3) * NP + b) * 128 + (tid & 7) * 16;
    __builtin_amdgcn_raw_buffer_store_b128(v0, rcell, co, 0, 16);
    __builtin_amdgcn_raw_buffer_store_b128(v1, rcell, co + 128 * NP * 128, 0, 16);
    STAMP(2);
  }
  {
    const rsrc_t rv = mk(wv, 32768);
    const u32 k0 = 8 * (w >> 1) + (g >> 1) + 4 * (g & 1) + 2 * (w & 1);
    dma16(rv, k0 * 256 + wsw, lb + L_WV + w * 1024);
    dma16(rv, (k0 + 64) * 256 + wsw, lb + L_WV + 16384 + w * 1024);
  }
  if (prod) {
    asm volatile("s_waitcnt vmcnt(4)" ::: "memory");
  } else {
    dma4(rx, (((b - NP) >> 3) * 1024 + tid) * 128, lb + L_STAGE + w * 256);
    asm volatile("s_waitcnt vmcnt(3)" ::: "memory");
  }
  asm volatile("" : "+v"(tag));
  tag = (tag + 0x100000u) & 0xFFF00000u;
  barrier_lds();
  STAMP(3);

  {
    const u32 nt = w & 7, q = (lane >> 2) & 3, p = lane & 3;
    u32x4 A[4];
#pragma unroll
    for (int t = 0; t < 2; ++t) {
      const u32 a = lb + L_WQ + (8 * g + 4 * t + q) * 256 + (((2 * nt + (p >> 1)) ^ ((q << 2) | ((2 * g + t) & 3))) * 16) + 8 * (p & 1);
#pragma unroll
      for (int s = 0; s < 4; ++s) { const u32x2 v = trread(a + s * 8192); A[s][2 * t] = v[0]; A[s][2 * t + 1] = v[1]; }
    }
    for (u32 mt = w >> 3; mt < 3; mt += 2) {
      f32x4 acc = {0, 0, 0, 0};
#pragma unroll
      for (int s = 0; s < 4; ++s) acc = mfma16(A[s], LDS128(lb + L_XO + (16 * mt + c) * 256 + (((4 * s + g) ^ c) * 16)), acc);
      LDS64(lb + L_Q + (16 * mt + c) * RSTR + nt * 32 + g * 8) = u32x2{pk_bf16(acc[0], acc[1]), pk_bf16(acc[2], acc[3])};
    }
  }
  STAMP(4);

  {
    u32x4 cc;
    const u32 co = b * (NP * 128) + tid * 16;
    u32 spins = 0;
    bool bad = false;
    for (;;) {
      asm volatile("" ::: "memory");
      cc = __builtin_amdgcn_raw_buffer_load_b128(rcell, co, 0, 16);
      cc ^= tag;
      if (((cc[0] | cc[1] | cc[2] | cc[3]) >> 20) == 0) break;
      if (++spins > (1u << 20)) { bad = true; cc = u32x4{EMPTY, EMPTY, EMPTY, EMPTY}; break; }
      __builtin_amdgcn_s_sleep(2);
    }
    STAMP(5);
    u32 pos[4];
#pragma unroll
    for (int k = 0; k < 4; ++k) {
      pos[k] = CAP;
      bad |= cc[k] == OVF;
      if (cc[k] < OVF) { pos[k] = __hip_atomic_fetch_add((LAS u32*)(lb + L_CNT + (cc[k] & 63) * 4), 1u, __ATOMIC_RELAXED, __HIP_MEMORY_SCOPE_WORKGROUP); bad |= pos[k] >= CAP; }
    }
#pragma unroll
    for (int k = 0; k < 4; ++k)
      if (pos[k] < CAP) LDS32(lb + L_LIST + ((cc[k] & 63) * LSTR + (pos[k] & 3) * 16 + (pos[k] >> 2)) * 4) = (cc[k] >> 6) << 8;
    if (bad) LDS32(lb + L_CNT + 63 * 4) = 1;
  }
  asm volatile("s_waitcnt vmcnt(0)" ::: "memory");
  barrier_lds();
  STAMP(6);
  if (tid == 0) __builtin_amdgcn_raw_buffer_store_b128(tag, mk(ws + WS_NONCE, NB * 16), b * 16, 0, 16);

  const u32 nn = ((u32)N - b + 255) >> 8;
  constexpr float KS = -0.08838834764831845f * 1.4426950408889634f;
  if (rfl(LDS32(lb + L_CNT + 63 * 4)) == 0) {
    const u32 e2 = (lane >> 2) & 3, q = lane & 3;
    const u32 loff = (4 * g + q) * 16;
    const u32 m0 = q == 0 ? 0xFFFFu : q == 1 ? 0xFFFF0000u : 0u, m1 = q == 2 ? 0xFFFFu : q == 3 ? 0xFFFF0000u : 0u;
    for (u32 dl = w; dl < nn; dl += 16) {
      const u32 n4 = (rfl(LDS32(lb + L_CNT + dl * 4)) + 3) >> 2;
      const u32x4 qf = LDS128(lb + L_Q + dl * RSTR + loff);
      f32x4 acc0 = {0, 0, 0, 0}, acc1 = {0, 0, 0, 0};
      for (u32 s0 = 0; s0 < n4; s0 += 12) {
        const u32 rem = n4 - s0;
        const u32 la = lb + L_LIST + (dl * LSTR + e2 * 16 + s0) * 4;
        const u32x4 en0 = LDS128(la), en1 = LDS128(la + 16), en2 = LDS128(la + 32);
        u32x4 X0, X1, X2, X3, X4, X5, X6, X7, X8, X9, X10, X11;
#define LD(J, EN, K) if (J < rem) { X##J = __builtin_amdgcn_raw_buffer_load_b128(rx, EN[K] + loff, 0, 0);
        LD(0, en0, 0) LD(1, en0, 1) LD(2, en0, 2) LD(3, en0, 3) LD(4, en1, 0) LD(5, en1, 1) LD(6, en1, 2) LD(7, en1, 3)
        LD(8, en2, 0) LD(9, en2, 1) LD(10, en2, 2) LD(11, en2, 3) }}}}}}}}}}}}
#undef LD
        const u32x4 Z = {0, 0, 0, 0};
        const u32x4 A0 = e2 == 0 ? qf : Z, A1 = e2 == 1 ? qf : Z, A2 = e2 == 2 ? qf : Z, A3 = e2 == 3 ? qf : Z;
#define SD(J, T) if (J < rem) { S = mfma16(A##T, X##J, S);
#define AG(J, T) if (J < rem) { acc0 = mfma4<T>(X##J[0], X##J[1], B, acc0); acc1 = mfma4<T>(X##J[2], X##J[3], B, acc1);
#define STEP(J0, J1, J2, J3) if (J0 < rem) { f32x4 S = {0, 0, 0, 0}; \
          SD(J0, 0) SD(J1, 1) SD(J2, 2) SD(J3, 3) }}}} \
          const float lg = (dppf<0x00>(S[0]) + dppf<0x55>(S[1])) + (dppf<0xAA>(S[2]) + dppf<0xFF>(S[3])); \
          const float al = __builtin_amdgcn_rcpf(1.0f + __builtin_amdgcn_exp2f(lg * KS)); \
          const u32 aa = pk_bf16(al, al); \
          const u32x2 B = {aa & m0, aa & m1}; \
          AG(J0, 0) AG(J1, 1) AG(J2, 2) AG(J3, 3) }}}}
        STEP(0, 1, 2, 3) STEP(4, 5, 6, 7) STEP(8, 9, 10, 11) }}}
#undef STEP
#undef AG
#undef SD
      }
      float y[2];
#pragma unroll
      for (int h = 0; h < 2; ++h) {
        const f32x4 a = h ? acc1 : acc0;
        const bool hi = e2 & 2, od = e2 & 1;
        const float k0 = hi ? a[2] : a[0], k1 = hi ? a[3] : a[1], t0 = hi ? a[0] : a[2], t1 = hi ? a[1] : a[3];
        const float u0 = k0 + dppf<0x128>(t0), u1 = k1 + dppf<0x128>(t1);
        const float k = od ? u1 : u0, t = od ? u0 : u1;
        y[h] = k + dppf<0x1B>(dppf<0x141>(t));
      }
      LDS32(lb + L_Y + dl * RSTR + lane * 4) = pk_bf16(y[0], y[1]);
    }
  } else {
    for (u32 i = tid; i < MAXN * 128; i += 1024) LDS32(lb + L_STAGE + i * 4) = 0;
    barrier_lds();
    for (u32 e0 = w * 64; e0 < (u32)E; e0 += 1024) {
      const u32 e = e0 + lane;
      const u32 d = e < (u32)E ? dst[e] : ~b, s = e < (u32)E ? src[e] : 0;
      unsigned long long m = __ballot((d & 255) == b);
      while (m) {
        const int j = __builtin_ctzll(m); m &= m - 1;
        const u32 sj = __builtin_amdgcn_readlane(s, j), dl = __builtin_amdgcn_readlane(d, j) >> 8;
        const u32 xv = ((const u32*)x)[sj * 64 + lane], qv = LDS32(lb + L_Q + dl * RSTR + lane * 4);
        const float x0 = __uint_as_float(xv << 16), x1 = __uint_as_float(xv & 0xFFFF0000u);
        float p = x0 * __uint_as_float(qv << 16) + x1 * __uint_as_float(qv & 0xFFFF0000u);
#pragma unroll
        for (int o = 32; o; o >>= 1) p += __shfl_xor(p, o);
        float al = __builtin_amdgcn_rcpf(1.0f + __builtin_amdgcn_exp2f(p * KS));
        al = __uint_as_float(pk_bf16(al, al) << 16);
        __hip_atomic_fetch_add((LAS float*)(lb + L_STAGE + (dl * 128 + 2 * lane) * 4), al * x0, __ATOMIC_RELAXED, __HIP_MEMORY_SCOPE_WORKGROUP);
        __hip_atomic_fetch_add((LAS float*)(lb + L_STAGE + (dl * 128 + 2 * lane + 1) * 4), al * x1, __ATOMIC_RELAXED, __HIP_MEMORY_SCOPE_WORKGROUP);
      }
    }
    barrier_lds();
    for (u32 dl = w; dl < nn; dl += 16) {
      const u32 k = (lane >> 2) * 8 + (lane & 3);
      LDS32(lb + L_Y + dl * RSTR + lane * 4) = pk_bf16(__uint_as_float(LDS32(lb + L_STAGE + (dl * 128 + k) * 4)), __uint_as_float(LDS32(lb + L_STAGE + (dl * 128 + k + 4) * 4)));
    }
  }
  STAMP(7);

  if (w < 8) {
    barrier_lds();
    FLUSH();
  } else {
    const u32 cg = w & 3, q = (lane >> 2) & 3, p = lane & 3;
    u32x4 B[2][4];
#pragma unroll
    for (int t = 0; t < 2; ++t) {
      const u32 a = lb + L_WV + (8 * g + 4 * t + q) * 256 + (((4 * cg + p) ^ ((q << 2) | ((2 * g + t) & 3))) * 16);
#pragma unroll
      for (int s = 0; s < 4; ++s)
#pragma unroll
        for (int m = 0; m < 2; ++m) { const u32x2 v = trread(a + s * 8192 + m * 8); B[m][s][2 * t] = v[0]; B[m][s][2 * t + 1] = v[1]; }
    }
    barrier_lds();
    STAMP(8);
    const rsrc_t ro = mk(out, N * 256);
    const u32 sel = p < 2 ? 0x05040100u : 0x07060302u;
    for (u32 mt = (w >> 2) & 1; mt < 3; mt += 2) {
      f32x4 o0 = {0, 0, 0, 0}, o1 = {0, 0, 0, 0};
#pragma unroll
      for (int s = 0; s < 4; ++s) {
        const u32x4 a = LDS128(lb + L_Y + (16 * mt + c) * RSTR + (4 * s + g) * 16);
        o0 = mfma16(a, B[0][s], o0);
        o1 = mfma16(a, B[1][s], o1);
      }
#pragma unroll
      for (int r = 0; r < 4; ++r) {
        const u32 pk = pk_bf16(o0[r], o1[r]);
        const u32 v = __builtin_amdgcn_perm(dppu<0xDD>(pk), dppu<0x88>(pk), sel);
        __builtin_amdgcn_raw_buffer_store_b32(v, ro, (b + 256 * (16 * mt + 4 * g + r)) * 256 + cg * 64 + c * 4, 0, 0);
      }
    }
    STAMP(9);
  }
}

extern "C" void kernel_launch(void* const* d_in, const int* in_sizes, int n_in, void* d_out, int out_size, void* d_ws, size_t ws_size,
                              hipStream_t stream) {
  const uint16_t* x = (const uint16_t*)d_in[0];
  const int* src = (const int*)d_in[1];
  const int* dst = (const int*)d_in[2];
  const uint16_t* wq = (const uint16_t*)d_in[3];
  const uint16_t* wv = (const uint16_t*)d_in[4];
  const int N = in_sizes[0] / 128, E = in_sizes[1];
  fg_fused<<<NB, 1024, 0, stream>>>(x, src, dst, wq, wv, (uint16_t*)d_out, (char*)d_ws, N, E
                                    );
}
```

```cpp
#define PADK 0
#include <hip/hip_runtime.h>
#include <hip/hip_bf16.h>
#include <stdint.h>

typedef unsigned u32;
typedef u32 u32x2 __attribute__((ext_vector_type(2)));
typedef u32 u32x4 __attribute__((ext_vector_type(4)));
typedef float f32x2 __attribute__((ext_vector_type(2)));
typedef float f32x4 __attribute__((ext_vector_type(4)));
typedef short s16x4 __attribute__((ext_vector_type(4)));
typedef __bf16 bf16x2 __attribute__((ext_vector_type(2)));
typedef __bf16 bf16x8 __attribute__((ext_vector_type(8)));
typedef __amdgpu_buffer_rsrc_t rsrc_t;
#define LAS __attribute__((address_space(3)))

#define DIAG 0
#define STAMP(i) do { if ((i) == 0 || (i) == PADK) { unsigned long long t_; asm volatile("s_memrealtime %0\n\ts_waitcnt lgkmcnt(0)" : "=s"(t_) :: "memory"); if ((i) == 0) pt0 = ptk = (u32)t_; else ptk = (u32)t_; } } while (0)
#define FLUSH() do { const u32 lim = 3000 + 10 * (ptk - pt0); while ((u32)__builtin_amdgcn_s_memrealtime() - pt0 < lim) __builtin_amdgcn_s_sleep(1); } while (0)

constexpr int NB = 256;
constexpr int NP = 128;
constexpr int MAXN = 48;
constexpr int CAP = 64;
constexpr int LSTR = 68;
constexpr int RSTR = 272;
constexpr u32 EMPTY = 0xFFFFFu, OVF = 0xFFFFEu;
constexpr u32 OOB = 0x80000000u;

constexpr int L_WQ = 0;
constexpr int L_WV = 32768;
constexpr int L_STAGE = 65536;
constexpr int L_XO = 98304;
constexpr int L_Q = L_XO + MAXN * 256;
constexpr int L_Y = L_Q + MAXN * RSTR;
constexpr int L_LIST = L_Y + MAXN * RSTR;
constexpr int L_CNT = L_LIST + MAXN * LSTR * 4;
constexpr int L_PCNT = L_CNT + 256;
constexpr int L_END = L_PCNT + 1024;

constexpr size_t WS_NONCE = 0;
constexpr size_t WS_CELLS = 4096;
constexpr size_t WS_END = WS_CELLS + (size_t)NB * NP * 128;

__device__ __forceinline__ u32 rfl(u32 v) { return __builtin_amdgcn_readfirstlane(v); }
__device__ __forceinline__ rsrc_t mk(const void* p, u32 bytes) { return __builtin_amdgcn_make_buffer_rsrc((void*)p, (short)0, (int)bytes, 0x00020000); }
__device__ __forceinline__ void dma16(rsrc_t r, u32 voff, u32 lds) {
  u32 keep;
  asm volatile("s_mov_b32 %0, m0\n\ts_mov_b32 m0, %3\n\ts_nop 0\n\tbuffer_load_dwordx4 %1, %2, 0 offen lds\n\ts_mov_b32 m0, %0"
               : "=&s"(keep) : "v"(voff), "s"(r), "s"(lds) : "memory");
}
__device__ __forceinline__ void dma4(rsrc_t r, u32 voff, u32 lds) {
  u32 keep;
  asm volatile("s_mov_b32 %0, m0\n\ts_mov_b32 m0, %3\n\ts_nop 0\n\tbuffer_load_dword %1, %2, 0 offen lds\n\ts_mov_b32 m0, %0"
               : "=&s"(keep) : "v"(voff), "s"(r), "s"(lds) : "memory");
}
__device__ __forceinline__ u32x4 ld16_async(rsrc_t r, u32 voff) {
  u32x4 v;
  asm volatile("buffer_load_dwordx4 %0, %1, %2, 0 offen" : "=v"(v) : "v"(voff), "s"(r) : "memory");
  return v;
}
__device__ __forceinline__ void barrier_lds() {
  asm volatile("s_waitcnt lgkmcnt(0)\n\ts_barrier" ::: "memory");
}
__device__ __forceinline__ u32 pk_bf16(float a, float b) {
  f32x2 f = {a, b};
  return __builtin_bit_cast(u32, __builtin_convertvector(f, bf16x2));
}
template <int CTRL> __device__ __forceinline__ float dppf(float v) {
  return __builtin_bit_cast(float, __builtin_amdgcn_update_dpp(0, __builtin_bit_cast(int, v), CTRL, 0xF, 0xF, true));
}
template <int CTRL> __device__ __forceinline__ u32 dppu(u32 v) {
  return (u32)__builtin_amdgcn_update_dpp(0, (int)v, CTRL, 0xF, 0xF, true);
}
__device__ __forceinline__ u32x2 trread(u32 a) {
  return __builtin_bit_cast(u32x2, __builtin_amdgcn_ds_read_tr16_b64_v4i16((LAS s16x4*)a));
}
__device__ __forceinline__ f32x4 mfma16(u32x4 a, u32x4 b, f32x4 c) {
  return __builtin_amdgcn_mfma_f32_16x16x32_bf16(__builtin_bit_cast(bf16x8, a), __builtin_bit_cast(bf16x8, b), c, 0, 0, 0);
}
template <int T> __device__ __forceinline__ f32x4 mfma4(u32 a0, u32 a1, u32x2 b, f32x4 c) {
  u32x2 a = {a0, a1};
  return __builtin_amdgcn_mfma_f32_4x4x4bf16_1k(__builtin_bit_cast(s16x4, a), __builtin_bit_cast(s16x4, b), c, 0, 0, 4 + T);
}
#define LDS32(a) (*(LAS u32*)(a))
#define LDS64(a) (*(LAS u32x2*)(a))
#define LDS128(a) (*(LAS u32x4*)(a))

__global__ __launch_bounds__(1024) void fg_fused(const uint16_t* __restrict__ x, const int* __restrict__ src, const int* __restrict__ dst,
                                                 const uint16_t* __restrict__ wq, const uint16_t* __restrict__ wv, uint16_t* __restrict__ out,
                                                 char* __restrict__ ws, int N, int E
                                                 ) {
  __shared__ __attribute__((aligned(16))) char smem[L_END];
  const u32 lb = (u32)(size_t)(LAS char*)smem;
  const u32 tid = threadIdx.x, lane = tid & 63, w = rfl(tid >> 6), b = blockIdx.x;
  const bool prod = b < NP;
  u32 pt0, ptk;
  STAMP(0);
  const rsrc_t rx = mk(x, N * 256);
  const rsrc_t rcell = mk(ws + WS_CELLS, NB * NP * 128);
  const u32 epp = ((E >> 2) + NP - 1) / NP;

  u32x4 d4, s4, n0, n1;
  const u32 grp = b * epp + tid;
  const bool has = tid < epp && grp < (u32)(E >> 2);
  if (prod) {
    const u32 eo = has ? grp * 16 : OOB;
    d4 = ld16_async(mk(dst, E * 4), eo);
    s4 = ld16_async(mk(src, E * 4), eo);
    const rsrc_t rn = mk(ws + WS_NONCE, NB * 16);
    n0 = ld16_async(rn, (tid >> 3) * 16);
    n1 = ld16_async(rn, (tid >> 3) * 16 + 2048);
  }

  if (tid < MAXN * LSTR / 4) LDS128(lb + L_LIST + tid * 16) = u32x4{OOB, OOB, OOB, OOB};
  if (tid < 80) LDS128(lb + L_CNT + tid * 16) = u32x4{0, 0, 0, 0};
  if (prod) {
    LDS128(lb + L_STAGE + tid * 16) = u32x4{EMPTY, EMPTY, EMPTY, EMPTY};
    LDS128(lb + L_STAGE + 16384 + tid * 16) = u32x4{EMPTY, EMPTY, EMPTY, EMPTY};
  }
  barrier_lds();

  const u32 g = lane >> 4, c = lane & 15;
  const u32 wsw = (c ^ ((g << 2) | (w & 3))) * 16;
  u32x4 tag = ld16_async(mk(ws + WS_NONCE, NB * 16), b * 16);
  {
    const rsrc_t rq = mk(wq, 32768);
    dma16(rq, (4 * w + g) * 256 + wsw, lb + L_WQ + w * 1024);
    dma16(rq, (4 * w + g + 64) * 256 + wsw, lb + L_WQ + 16384 + w * 1024);
    const u32 r = 4 * w + g;
    dma16(rx, w < MAXN / 4 ? (b + 256 * r) * 256 + ((c ^ r) & 15) * 16 : OOB, w < MAXN / 4 ? lb + L_XO + w * 1024 : lb + L_Y);
  }
  if (prod) {
    asm volatile("s_waitcnt vmcnt(4)" : "+v"(d4), "+v"(s4), "+v"(n0), "+v"(n1) :: "memory");
    STAMP(1);
    if (has) {
      u32 r[4];
#pragma unroll
      for (int k = 0; k < 4; ++k) r[k] = __hip_atomic_fetch_add((LAS u32*)(lb + L_PCNT + (d4[k] & 255) * 4), 1u, __ATOMIC_RELAXED, __HIP_MEMORY_SCOPE_WORKGROUP);
#pragma unroll
      for (int k = 0; k < 4; ++k) {
        const u32 bk = d4[k] & 255, rr = min(r[k], 31u);
        const u32 idx = (((rr & 7) << 2) | (rr >> 3)) ^ (bk & 31);
        LDS32(lb + L_STAGE + bk * 128 + idx * 4) = r[k] >= 31u ? OVF : ((s4[k] << 6) | (d4[k] >> 8));
      }
    }
    barrier_lds();
    u32x4 v0 = LDS128(lb + L_STAGE + tid * 16), v1 = LDS128(lb + L_STAGE + 16384 + tid * 16);
    v0 |= (n0 + 0x100000u) & 0xFFF00000u;
    v1 |= (n1 + 0x100000u) & 0xFFF00000u;
    const u32 co = ((tid >> 3) * NP + b) * 128 + (tid & 7) * 16;
    __builtin_amdgcn_raw_buffer_store_b128(v0, rcell, co, 0, 16);
    __builtin_amdgcn_raw_buffer_store_b128(v1, rcell, co + 128 * NP * 128, 0, 16);
    STAMP(2);
  }
  {
    const rsrc_t rv = mk(wv, 32768);
    const u32 k0 = 8 * (w >> 1) + (g >> 1) + 4 * (g & 1) + 2 * (w & 1);
    dma16(rv, k0 * 256 + wsw, lb + L_WV + w * 1024);
    dma16(rv, (k0 + 64) * 256 + wsw, lb + L_WV + 16384 + w * 1024);
  }
  if (prod) {
    asm volatile("s_waitcnt vmcnt(4)" ::: "memory");
  } else {
    dma4(rx, (((b - NP) >> 3) * 1024 + tid) * 128, lb + L_STAGE + w * 256);
    asm volatile("s_waitcnt vmcnt(3)" ::: "memory");
  }
  asm volatile("" : "+v"(tag));
  tag = (tag + 0x100000u) & 0xFFF00000u;
  barrier_lds();
  STAMP(3);

  {
    const u32 nt = w & 7, q = (lane >> 2) & 3, p = lane & 3;
    u32x4 A[4];
#pragma unroll
    for (int t = 0; t < 2; ++t) {
      const u32 a = lb + L_WQ + (8 * g + 4 * t + q) * 256 + (((2 * nt + (p >> 1)) ^ ((q << 2) | ((2 * g + t) & 3))) * 16) + 8 * (p & 1);
#pragma unroll
      for (int s = 0; s < 4; ++s) { const u32x2 v = trread(a + s * 8192); A[s][2 * t] = v[0]; A[s][2 * t + 1] = v[1]; }
    }
    for (u32 mt = w >> 3; mt < 3; mt += 2) {
      f32x4 acc = {0, 0, 0, 0};
#pragma unroll
      for (int s = 0; s < 4; ++s) acc = mfma16(A[s], LDS128(lb + L_XO + (16 * mt + c) * 256 + (((4 * s + g) ^ c) * 16)), acc);
      LDS64(lb + L_Q + (16 * mt + c) * RSTR + nt * 32 + g * 8) = u32x2{pk_bf16(acc[0], acc[1]), pk_bf16(acc[2], acc[3])};
    }
  }
  STAMP(4);

  {
    u32x4 cc;
    const u32 co = b * (NP * 128) + tid * 16;
    u32 spins = 0;
    bool bad = false;
    for (;;) {
      asm volatile("" ::: "memory");
      cc = __builtin_amdgcn_raw_buffer_load_b128(rcell, co, 0, 16);
      cc ^= tag;
      if (((cc[0] | cc[1] | cc[2] | cc[3]) >> 20) == 0) break;
      if (++spins > (1u << 20)) { bad = true; cc = u32x4{EMPTY, EMPTY, EMPTY, EMPTY}; break; }
      __builtin_amdgcn_s_sleep(2);
    }
    STAMP(5);
    u32 pos[4];
#pragma unroll
    for (int k = 0; k < 4; ++k) {
      pos[k] = CAP;
      bad |= cc[k] == OVF;
      if (cc[k] < OVF) { pos[k] = __hip_atomic_fetch_add((LAS u32*)(lb + L_CNT + (cc[k] & 63) * 4), 1u, __ATOMIC_RELAXED, __HIP_MEMORY_SCOPE_WORKGROUP); bad |= pos[k] >= CAP; }
    }
#pragma unroll
    for (int k = 0; k < 4; ++k)
      if (pos[k] < CAP) LDS32(lb + L_LIST + ((cc[k] & 63) * LSTR + (pos[k] & 3) * 16 + (pos[k] >> 2)) * 4) = (cc[k] >> 6) << 8;
    if (bad) LDS32(lb + L_CNT + 63 * 4) = 1;
  }
  asm volatile("s_waitcnt vmcnt(0)" ::: "memory");
  barrier_lds();
  STAMP(6);
  if (tid == 0) __builtin_amdgcn_raw_buffer_store_b128(tag, mk(ws + WS_NONCE, NB * 16), b * 16, 0, 16);

  const u32 nn = ((u32)N - b + 255) >> 8;
  constexpr float KS = -0.08838834764831845f * 1.4426950408889634f;
  if (rfl(LDS32(lb + L_CNT + 63 * 4)) == 0) {
    const u32 e2 = (lane >> 2) & 3, q = lane & 3;
    const u32 loff = (4 * g + q) * 16;
    const u32 m0 = q == 0 ? 0xFFFFu : q == 1 ? 0xFFFF0000u : 0u, m1 = q == 2 ? 0xFFFFu : q == 3 ? 0xFFFF0000u : 0u;
    for (u32 dl = w; dl < nn; dl += 16) {
      const u32 n4 = (rfl(LDS32(lb + L_CNT + dl * 4)) + 3) >> 2;
      const u32x4 qf = LDS128(lb + L_Q + dl * RSTR + loff);
      f32x4 acc0 = {0, 0, 0, 0}, acc1 = {0, 0, 0, 0};
      for (u32 s0 = 0; s0 < n4; s0 += 12) {
        const u32 rem = n4 - s0;
        const u32 la = lb + L_LIST + (dl * LSTR + e2 * 16 + s0) * 4;
        const u32x4 en0 = LDS128(la), en1 = LDS128(la + 16), en2 = LDS128(la + 32);
        u32x4 X0, X1, X2, X3, X4, X5, X6, X7, X8, X9, X10, X11;
#define LD(J, EN, K) if (J < rem) { X##J = __builtin_amdgcn_raw_buffer_load_b128(rx, EN[K] + loff, 0, 0);
        LD(0, en0, 0) LD(1, en0, 1) LD(2, en0, 2) LD(3, en0, 3) LD(4, en1, 0) LD(5, en1, 1) LD(6, en1, 2) LD(7, en1, 3)
        LD(8, en2, 0) LD(9, en2, 1) LD(10, en2, 2) LD(11, en2, 3) }}}}}}}}}}}}
#undef LD
        const u32x4 Z = {0, 0, 0, 0};
        const u32x4 A0 = e2 == 0 ? qf : Z, A1 = e2 == 1 ? qf : Z, A2 = e2 == 2 ? qf : Z, A3 = e2 == 3 ? qf : Z;
#define SD(J, T) if (J < rem) { S = mfma16(A##T, X##J, S);
#define AG(J, T) if (J < rem) { acc0 = mfma4<T>(X##J[0], X##J[1], B, acc0); acc1 = mfma4<T>(X##J[2], X##J[3], B, acc1);
#define STEP(J0, J1, J2, J3) if (J0 < rem) { f32x4 S = {0, 0, 0, 0}; \
          SD(J0, 0) SD(J1, 1) SD(J2, 2) SD(J3, 3) }}}} \
          const float lg = (dppf<0x00>(S[0]) + dppf<0x55>(S[1])) + (dppf<0xAA>(S[2]) + dppf<0xFF>(S[3])); \
          const float al = __builtin_amdgcn_rcpf(1.0f + __builtin_amdgcn_exp2f(lg * KS)); \
          const u32 aa = pk_bf16(al, al); \
          const u32x2 B = {aa & m0, aa & m1}; \
          AG(J0, 0) AG(J1, 1) AG(J2, 2) AG(J3, 3) }}}}
        STEP(0, 1, 2, 3) STEP(4, 5, 6, 7) STEP(8, 9, 10, 11) }}}
#undef STEP
#undef AG
#undef SD
      }
      float y[2];
#pragma unroll
      for (int h = 0; h < 2; ++h) {
        const f32x4 a = h ? acc1 : acc0;
        const bool hi = e2 & 2, od = e2 & 1;
        const float k0 = hi ? a[2] : a[0], k1 = hi ? a[3] : a[1], t0 = hi ? a[0] : a[2], t1 = hi ? a[1] : a[3];
        const float u0 = k0 + dppf<0x128>(t0), u1 = k1 + dppf<0x128>(t1);
        const float k = od ? u1 : u0, t = od ? u0 : u1;
        y[h] = k + dppf<0x1B>(dppf<0x141>(t));
      }
      LDS32(lb + L_Y + dl * RSTR + lane * 4) = pk_bf16(y[0], y[1]);
    }
  } else {
    for (u32 i = tid; i < MAXN * 128; i += 1024) LDS32(lb + L_STAGE + i * 4) = 0;
    barrier_lds();
    for (u32 e0 = w * 64; e0 < (u32)E; e0 += 1024) {
      const u32 e = e0 + lane;
      const u32 d = e < (u32)E ? dst[e] : ~b, s = e < (u32)E ? src[e] : 0;
      unsigned long long m = __ballot((d & 255) == b);
      while (m) {
        const int j = __builtin_ctzll(m); m &= m - 1;
        const u32 sj = __builtin_amdgcn_readlane(s, j), dl = __builtin_amdgcn_readlane(d, j) >> 8;
        const u32 xv = ((const u32*)x)[sj * 64 + lane], qv = LDS32(lb + L_Q + dl * RSTR + lane * 4);
        const float x0 = __uint_as_float(xv << 16), x1 = __uint_as_float(xv & 0xFFFF0000u);
        float p = x0 * __uint_as_float(qv << 16) + x1 * __uint_as_float(qv & 0xFFFF0000u);
#pragma unroll
        for (int o = 32; o; o >>= 1) p += __shfl_xor(p, o);
        float al = __builtin_amdgcn_rcpf(1.0f + __builtin_amdgcn_exp2f(p * KS));
        al = __uint_as_float(pk_bf16(al, al) << 16);
        __hip_atomic_fetch_add((LAS float*)(lb + L_STAGE + (dl * 128 + 2 * lane) * 4), al * x0, __ATOMIC_RELAXED, __HIP_MEMORY_SCOPE_WORKGROUP);
        __hip_atomic_fetch_add((LAS float*)(lb + L_STAGE + (dl * 128 + 2 * lane + 1) * 4), al * x1, __ATOMIC_RELAXED, __HIP_MEMORY_SCOPE_WORKGROUP);
      }
    }
    barrier_lds();
    for (u32 dl = w; dl < nn; dl += 16) {
      const u32 k = (lane >> 2) * 8 + (lane & 3);
      LDS32(lb + L_Y + dl * RSTR + lane * 4) = pk_bf16(__uint_as_float(LDS32(lb + L_STAGE + (dl * 128 + k) * 4)), __uint_as_float(LDS32(lb + L_STAGE + (dl * 128 + k + 4) * 4)));
    }
  }
  STAMP(7);

  if (w < 8) {
    barrier_lds();
    FLUSH();
  } else {
    const u32 cg = w & 3, q = (lane >> 2) & 3, p = lane & 3;
    u32x4 B[2][4];
#pragma unroll
    for (int t = 0; t < 2; ++t) {
      const u32 a = lb + L_WV + (8 * g + 4 * t + q) * 256 + (((4 * cg + p) ^ ((q << 2) | ((2 * g + t) & 3))) * 16);
#pragma unroll
      for (int s = 0; s < 4; ++s)
#pragma unroll
        for (int m = 0; m < 2; ++m) { const u32x2 v = trread(a + s * 8192 + m * 8); B[m][s][2 * t] = v[0]; B[m][s][2 * t + 1] = v[1]; }
    }
    barrier_lds();
    STAMP(8);
    const rsrc_t ro = mk(out, N * 256);
    const u32 sel = p < 2 ? 0x05040100u : 0x07060302u;
    for (u32 mt = (w >> 2) & 1; mt < 3; mt += 2) {
      f32x4 o0 = {0, 0, 0, 0}, o1 = {0, 0, 0, 0};
#pragma unroll
      for (int s = 0; s < 4; ++s) {
        const u32x4 a = LDS128(lb + L_Y + (16 * mt + c) * RSTR + (4 * s + g) * 16);
        o0 = mfma16(a, B[0][s], o0);
        o1 = mfma16(a, B[1][s], o1);
      }
#pragma unroll
      for (int r = 0; r < 4; ++r) {
        const u32 pk = pk_bf16(o0[r], o1[r]);
        const u32 v = __builtin_amdgcn_perm(dppu<0xDD>(pk), dppu<0x88>(pk), sel);
        __builtin_amdgcn_raw_buffer_store_b32(v, ro, (b + 256 * (16 * mt + 4 * g + r)) * 256 + cg * 64 + c * 4, 0, 0);
      }
    }
    STAMP(9);
    asm volatile("s_waitcnt vmcnt(0)" ::: "memory");
    STAMP(10);
    FLUSH();
  }
}

extern "C" void kernel_launch(void* const* d_in, const int* in_sizes, int n_in, void* d_out, int out_size, void* d_ws, size_t ws_size,
                              hipStream_t stream) {
  const uint16_t* x = (const uint16_t*)d_in[0];
  const int* src = (const int*)d_in[1];
  const int* dst = (const int*)d_in[2];
  const uint16_t* wq = (const uint16_t*)d_in[3];
  const uint16_t* wv = (const uint16_t*)d_in[4];
  const int N = in_sizes[0] / 128, E = in_sizes[1];
  fg_fused<<<NB, 1024, 0, stream>>>(x, src, dst, wq, wv, (uint16_t*)d_out, (char*)d_ws, N, E
                                    );
}
```

```cpp
#include <hip/hip_runtime.h>
#include <hip/hip_bf16.h>
#include <stdint.h>

typedef unsigned u32;
typedef float f32x2 __attribute__((ext_vector_type(2)));
typedef __bf16 bf16x2 __attribute__((ext_vector_type(2)));
#define LAS __attribute__((address_space(3)))
#define LDS32(a) (*(LAS u32*)(a))

constexpr u32 NN = 10000, EE = 320000;
constexpr int L_Q = 54528, L_STAGE = 13568, L_END = 158464, RSTR = 272;

__device__ __forceinline__ u32 pk_bf16(float a, float b) {
  f32x2 f = {a, b};
  return __builtin_bit_cast(u32, __builtin_convertvector(f, bf16x2));
}

__global__ __launch_bounds__(1024) __attribute__((aligned(4096))) void fg_fused(const int* __restrict__ dst, const int* __restrict__ src, char* __restrict__ ws,
                                                 const uint16_t* __restrict__ x, const uint16_t* __restrict__ wq, const uint16_t* __restrict__ wv,
                                                 uint16_t* __restrict__ out) {
  __shared__ __attribute__((aligned(16))) char smem[L_END];
  const u32 tid = threadIdx.x, wg = blockIdx.x;
  asm volatile(
    "s_memrealtime s[98:99]\n\t"
    "s_load_dwordx8 s[8:15], %[ka], 0x0\n\t"
    "s_load_dwordx4 s[16:19], %[ka], 0x20\n\t"
    "s_load_dwordx2 s[20:21], %[ka], 0x30\n\t"
    "s_mov_b32 s4, 4194111\n\t"
    "s_mov_b32 s5, 4194111\n\t"
    "v_lshlrev_b32 v15, 4, %[tid]\n\t"
    "v_lshrrev_b32 v26, 3, %[tid]\n\t"
    "v_mov_b64 v[112:113], s[4:5]\n\t"
    "v_mov_b64 v[114:115], s[4:5]\n\t"
    "v_readfirstlane_b32 s22, %[tid]\n\t"
    "s_cmp_ge_u32 %[b], 128\n\t"
    "s_cbranch_scc1 .Lp0_%=\n\t"
    "v_lshl_add_u32 v29, v26, 5, v15\n\t"
    "ds_write_b128 v29, v[112:115] offset:13568\n\t"
    "ds_write_b128 v29, v[112:115] offset:34048\n\t"
    ".Lp0_%=:\n\t"
    "v_mov_b64 v[8:9], 0\n\t"
    "s_lshr_b32 s22, s22, 6\n\t"
    "s_mov_b32 s59, 4290773184\n\t"
    "s_cmp_lt_u32 s22, 8\n\t"
    "s_cbranch_scc1 .Lawv_%=\n\t"
    "v_cmp_gt_u32 vcc, 672, %[tid]\n\t"
    "v_lshlrev_b32 v24, 3, %[tid]\n\t"
    "v_subrev_u32 v24, 4096, v24\n\t"
    "s_mov_b64 exec, vcc\n\t"
    "ds_write_b64 v24, v[8:9]\n\t"
    "s_mov_b64 exec, -1\n\t"
    "s_waitcnt lgkmcnt(0)\n\t"
    "s_barrier\n\t"
    "s_load_dwordx4 s[36:39], s[12:13], 0x0\n\t"
    "v_bfe_u32 v52, %[tid], 4, 2\n\t"
    "v_and_b32 v18, 0xf0, v15\n\t"
    "s_and_b32 s24, s22, 3\n\t"
    "s_lshl_b32 s23, s22, 7\n\t"
    "s_and_b32 s23, s23, 0x80\n\t"
    "s_lshl_b32 s25, s24, 11\n\t"
    "v_lshlrev_b32 v53, 5, v52\n\t"
    "s_or_b32 s23, s23, s25\n\t"
    "s_bfe_u32 s25, s22, 0x10002\n\t"
    "s_lshl_b32 s25, s25, 14\n\t"
    "s_or_b32 s23, s23, s25\n\t"
    "s_lshl_b32 s24, s24, 10\n\t"
    "s_or_b32 s26, s24, s25\n\t"
    "v_bitop3_b32 v45, v18, v53, s23 bitop3:0x96\n\t"
    "v_lshl_add_u32 v51, v52, 8, v45\n\t"
    "global_load_dwordx4 v[64:67], v51, s[16:17]\n\t"
    "global_load_dwordx4 v[68:71], v51, s[16:17] offset:1024\n\t"
    "v_add_u32 v51, 0x2000, v51\n\t"
    "global_load_dwordx4 v[72:75], v51, s[16:17]\n\t"
    "global_load_dwordx4 v[76:79], v51, s[16:17] offset:1024\n\t"
    "s_cmp_eq_u32 s22, 15\n\t"
    "s_cbranch_scc1 .Ltouch0_%=\n\t"
    "s_cmp_eq_u32 s22, 14\n\t"
    "s_cbranch_scc1 .Ltouch1_%=\n\t"
    "s_cmp_eq_u32 s22, 13\n\t"
    "s_cbranch_scc1 .Ltouch2_%=\n\t"
    "s_cmp_eq_u32 s22, 12\n\t"
    "s_cbranch_scc1 .Ltouch3_%=\n\t"
    "s_cmp_eq_u32 s22, 11\n\t"
    "s_cbranch_scc1 .Ltouch4_%=\n\t"
    "s_cmp_eq_u32 s22, 10\n\t"
    "s_cbranch_scc1 .Ltouch5_%=\n\t"
    "s_branch .Lbw_%=\n\t"
    ".Lanp_%=:\n\t"
    "s_waitcnt lgkmcnt(0)\n\t"
    "s_barrier\n\t"
    "s_load_dwordx4 s[36:39], s[12:13], 0x0\n\t"
    "s_branch .Lawv2_%=\n\t"
    ".Lanp2_%=:\n\t"
    "s_cmp_ge_u32 s22, 3\n\t"
    "s_cbranch_scc1 .Lpfe_%=\n\t"
    "s_sub_u32 s23, %[b], 128\n\t"
    "s_lshr_b32 s23, s23, 3\n\t"
    "s_mul_i32 s23, s23, 24576\n\t"
    "s_lshl_b32 s24, s22, 8\n\t"
    "s_add_u32 m0, s24, 13568\n\t"
    "v_lshl_add_u32 v48, %[tid], 7, s23\n\t"
    "global_load_lds_dword v48, s[14:15]\n\t"
    ".Lpfe_%=:\n\t"
    "s_waitcnt lgkmcnt(0)\n\t"
    "s_add_u32 s36, s36, 0x01400000\n\t"
    "s_and_b32 s36, s36, s59\n\t"
    "s_add_u32 s37, s37, 0x01400000\n\t"
    "s_and_b32 s37, s37, s59\n\t"
    "s_add_u32 s38, s38, 0x01400000\n\t"
    "s_and_b32 s38, s38, s59\n\t"
    "s_add_u32 s39, s39, 0x01400000\n\t"
    "s_and_b32 s39, s39, s59\n\t"
    "s_barrier\n\t"
    "s_branch .Law2_%=\n\t"
    ".Lawv_%=:\n\t"
    "s_cmp_ge_u32 %[b], 128\n\t"
    "s_cbranch_scc1 .Lanp_%=\n\t"
    "s_mul_i32 s23, %[b], 10000\n\t"
    "v_cmp_gt_u32 s[40:41], %[cepp], %[tid]\n\t"
    "v_lshlrev_b32 v30, 2, %[tid]\n\t"
    "s_add_u32 s24, s23, 8000\n\t"
    "v_add_u32 v25, s23, v15\n\t"
    "v_add_u32 v24, s24, v30\n\t"
    "s_mov_b64 exec, s[40:41]\n\t"
    "s_waitcnt lgkmcnt(0)\n\t"
    "global_load_dwordx4 v[40:43], v25, s[8:9]\n\t"
    "global_load_dword v44, v24, s[8:9]\n\t"
    "global_load_dwordx4 v[46:49], v25, s[10:11]\n\t"
    "global_load_dword v50, v24, s[10:11]\n\t"
    "s_mov_b64 exec, -1\n\t"
    "s_barrier\n\t"
    "s_load_dwordx4 s[36:39], s[12:13], 0x0\n\t"
    "s_lshr_b32 s23, %[b], 3\n\t"
    "v_lshl_add_u32 v30, v26, 7, v30\n\t"
    "s_lshl_b32 s23, s23, 7\n\t"
    "v_add_u32 v31, 23808, v30\n\t"
    "v_add_u32 v32, 34048, v30\n\t"
    "v_add_u32 v33, 44288, v30\n\t"
    "v_add_u32 v30, 13568, v30\n\t"
    "v_add_u32 v26, s23, v15\n\t"
    "s_lshl_b32 s24, %[b], 15\n\t"
    "v_add_u32 v29, 0x6000, v26\n\t"
    "s_add_u32 s24, s24, 32768\n\t"
    "v_add_u32 v27, 0x2000, v26\n\t"
    "v_add_u32 v28, 0x4000, v26\n\t"
    "v_and_b32 v29, 0x7fff, v29\n\t"
    "v_mov_b32 v23, 4\n\t"
    "s_mov_b32 s23, 0x0C050401\n\t"
    "s_setprio 3\n\t"
    "s_add_u32 s60, s12, s24\n\t"
    "s_addc_u32 s61, s13, 0\n\t"
    "s_mov_b64 exec, s[40:41]\n\t"
    "s_waitcnt vmcnt(3)\n\t"
    "v_lshlrev_b32_sdwa v52, 2, v40 dst_sel:DWORD dst_unused:UNUSED_PAD src0_sel:DWORD src1_sel:BYTE_0\n\t"
    "ds_add_rtn_u32 v58, v52, v23\n\t"
    "v_lshlrev_b32_sdwa v53, 2, v41 dst_sel:DWORD dst_unused:UNUSED_PAD src0_sel:DWORD src1_sel:BYTE_0\n\t"
    "ds_add_rtn_u32 v59, v53, v23\n\t"
    "v_lshlrev_b32_sdwa v54, 2, v42 dst_sel:DWORD dst_unused:UNUSED_PAD src0_sel:DWORD src1_sel:BYTE_0\n\t"
    "ds_add_rtn_u32 v60, v54, v23\n\t"
    "v_lshlrev_b32_sdwa v55, 2, v43 dst_sel:DWORD dst_unused:UNUSED_PAD src0_sel:DWORD src1_sel:BYTE_0\n\t"
    "ds_add_rtn_u32 v61, v55, v23\n\t"
    "s_waitcnt vmcnt(2)\n\t"
    "v_lshlrev_b32_sdwa v56, 2, v44 dst_sel:DWORD dst_unused:UNUSED_PAD src0_sel:DWORD src1_sel:BYTE_0\n\t"
    "ds_add_rtn_u32 v62, v56, v23\n\t"
    "s_waitcnt vmcnt(0)\n\t"
    "v_perm_b32 v40, v46, v40, s23\n\t"
    "v_perm_b32 v41, v47, v41, s23\n\t"
    "v_perm_b32 v42, v48, v42, s23\n\t"
    "v_perm_b32 v43, v49, v43, s23\n\t"
    "v_perm_b32 v44, v50, v44, s23\n\t"
    "s_waitcnt lgkmcnt(0)\n\t"
    "v_min_u32 v58, 124, v58\n\t"
    "v_mad_u32_u24 v58, v52, 40, v58\n\t"
    "ds_write_b32 v58, v40 offset:13568\n\t"
    "v_min_u32 v59, 124, v59\n\t"
    "v_mad_u32_u24 v59, v53, 40, v59\n\t"
    "ds_write_b32 v59, v41 offset:13568\n\t"
    "v_min_u32 v60, 124, v60\n\t"
    "v_mad_u32_u24 v60, v54, 40, v60\n\t"
    "ds_write_b32 v60, v42 offset:13568\n\t"
    "v_min_u32 v61, 124, v61\n\t"
    "v_mad_u32_u24 v61, v55, 40, v61\n\t"
    "ds_write_b32 v61, v43 offset:13568\n\t"
    "v_min_u32 v62, 124, v62\n\t"
    "v_mad_u32_u24 v62, v56, 40, v62\n\t"
    "ds_write_b32 v62, v44 offset:13568\n\t"
    "s_mov_b64 exec, 1\n\t"
    "ds_add_u32 v8, v23 offset:1268\n\t"
    "s_mov_b64 exec, -1\n\t"
    ".Lspb_%=:\n\t"
    "ds_read_b32 v24, v8 offset:1268\n\t"
    "s_waitcnt lgkmcnt(0)\n\t"
    "v_cmp_gt_u32 vcc, 32, v24\n\t"
    "s_cbranch_vccnz .Lspb_%=\n\t"
    "ds_read2_b32 v[64:65], v30 offset0:0 offset1:8\n\t"
    "ds_read2_b32 v[66:67], v30 offset0:16 offset1:24\n\t"
    "ds_read2_b32 v[68:69], v31 offset0:0 offset1:8\n\t"
    "ds_read2_b32 v[70:71], v31 offset0:16 offset1:24\n\t"
    "ds_read2_b32 v[72:73], v32 offset0:0 offset1:8\n\t"
    "ds_read2_b32 v[74:75], v32 offset0:16 offset1:24\n\t"
    "ds_read2_b32 v[76:77], v33 offset0:0 offset1:8\n\t"
    "ds_read2_b32 v[78:79], v33 offset0:16 offset1:24\n\t"
    "s_add_u32 s36, s36, 0x01400000\n\t"
    "s_and_b32 s36, s36, s59\n\t"
    "s_add_u32 s37, s37, 0x01400000\n\t"
    "s_and_b32 s37, s37, s59\n\t"
    "s_add_u32 s38, s38, 0x01400000\n\t"
    "s_and_b32 s38, s38, s59\n\t"
    "s_add_u32 s39, s39, 0x01400000\n\t"
    "s_and_b32 s39, s39, s59\n\t"
    "s_waitcnt lgkmcnt(6)\n\t"
    "v_or_b32 v64, s36, v64\n\t"
    "v_or_b32 v65, s37, v65\n\t"
    "v_or_b32 v66, s38, v66\n\t"
    "v_or_b32 v67, s39, v67\n\t"
    "global_store_dwordx4 v26, v[64:67], s[60:61] sc1\n\t"
    "s_waitcnt lgkmcnt(4)\n\t"
    "v_or_b32 v68, s36, v68\n\t"
    "v_or_b32 v69, s37, v69\n\t"
    "v_or_b32 v70, s38, v70\n\t"
    "v_or_b32 v71, s39, v71\n\t"
    "global_store_dwordx4 v27, v[68:71], s[60:61] sc1\n\t"
    "s_waitcnt lgkmcnt(2)\n\t"
    "v_or_b32 v72, s36, v72\n\t"
    "v_or_b32 v73, s37, v73\n\t"
    "v_or_b32 v74, s38, v74\n\t"
    "v_or_b32 v75, s39, v75\n\t"
    "global_store_dwordx4 v28, v[72:75], s[60:61] sc1\n\t"
    "s_waitcnt lgkmcnt(0)\n\t"
    "v_or_b32 v76, s36, v76\n\t"
    "v_or_b32 v77, s37, v77\n\t"
    "v_or_b32 v78, s38, v78\n\t"
    "v_or_b32 v79, s39, v79\n\t"
    "global_store_dwordx4 v29, v[76:79], s[60:61] sc1\n\t"
    "s_setprio 0\n\t"
    "s_barrier\n\t"
    ".Law_%=:\n\t"
    ".Lawv2_%=:\n\t"
    "v_bfe_u32 v52, %[tid], 4, 2\n\t"
    "v_and_b32 v18, 0xf0, v15\n\t"
    "s_and_b32 s24, s22, 3\n\t"
    "s_lshl_b32 s23, s22, 7\n\t"
    "s_and_b32 s23, s23, 0x80\n\t"
    "s_lshl_b32 s25, s24, 11\n\t"
    "v_lshlrev_b32 v53, 5, v52\n\t"
    "s_or_b32 s23, s23, s25\n\t"
    "s_bfe_u32 s25, s22, 0x10002\n\t"
    "s_lshl_b32 s25, s25, 14\n\t"
    "s_or_b32 s23, s23, s25\n\t"
    "s_lshl_b32 s24, s24, 10\n\t"
    "s_or_b32 s26, s24, s25\n\t"
    "v_bitop3_b32 v45, v18, v53, s23 bitop3:0x96\n\t"
    "s_add_u32 m0, s26, 125696\n\t"
    "v_and_b32 v53, 1, v52\n\t"
    "v_lshrrev_b32 v54, 1, v52\n\t"
    "v_lshl_add_u32 v54, v53, 2, v54\n\t"
    "v_lshl_add_u32 v45, v54, 8, v45\n\t"
    "global_load_lds_dwordx4 v45, s[18:19]\n\t"
    "s_add_u32 m0, m0, 3584\n\t"
    "s_nop 0\n\t"
    "global_load_lds_dwordx4 v45, s[18:19] offset:512\n\t"
    "s_add_u32 m0, m0, 4608\n\t"
    "v_add_u32 v45, 0x2000, v45\n\t"
    "global_load_lds_dwordx4 v45, s[18:19]\n\t"
    "s_add_u32 m0, m0, 3584\n\t"
    "s_nop 0\n\t"
    "global_load_lds_dwordx4 v45, s[18:19] offset:512\n\t"
    "s_cmp_ge_u32 %[b], 128\n\t"
    "s_cbranch_scc1 .Lanp2_%=\n\t"
    ".Law2_%=:\n\t"
    "s_mov_b32 s28, s14\n\t"
    "s_and_b32 s29, s15, 0xffff\n\t"
    "s_mov_b32 s30, 2560000\n\t"
    "s_mov_b32 s31, 0x20000\n\t"
    "s_lshr_b32 s23, %[b], 3\n\t"
    "s_add_u32 s23, s23, %[b]\n\t"
    "s_and_b32 s23, s23, 7\n\t"
    "s_andn2_b32 s34, %[b], 7\n\t"
    "s_or_b32 s34, s34, s23\n\t"
    "v_and_b32 v20, 63, %[tid]\n\t"
    "s_mul_i32 s23, s22, 272\n\t"
    "v_lshlrev_b32 v14, 2, v20\n\t"
    "s_add_u32 s24, s23, 1280\n\t"
    "s_add_u32 s25, s23, 54528\n\t"
    "s_add_u32 s23, s23, 67584\n\t"
    "v_and_or_b32 v1, v20, 12, s24\n\t"
    "v_bfi_b32 v4, 63, v15, v14\n\t"
    "s_mov_b64 s[4:5], 0xffff\n\t"
    "v_add_u32 v3, s23, v14\n\t"
    "v_add_u32 v2, s25, v4\n\t"
    "v_lshlrev_b64 v[6:7], v15, s[4:5]\n\t"
    "v_mov_b32 v5, 1\n\t"
    "s_mov_b32 s48, 0x000f000f\n\t"
    "s_mov_b32 s49, 0x000f000f\n\t"
    "s_mov_b32 s94, 0x22222222\n\t"
    "s_mov_b32 s95, 0x22222222\n\t"
    "s_lshl_b64 s[96:97], s[94:95], 1\n\t"
    "s_mov_b32 s57, 0\n\t"
    "s_mov_b32 s55, 1\n\t"
    "s_getpc_b64 s[6:7]\n\t"
    ".Lpc0_%=:\n\t"
    "s_add_u32 s8, s6, .Lsend_%=-.Lpc0_%=\n\t"
    "s_addc_u32 s9, s7, 0\n\t"
    "s_add_u32 s10, s6, .Laend_%=-.Lpc0_%=\n\t"
    "s_addc_u32 s11, s7, 0\n\t"
    "s_add_u32 s92, s6, .Llend_%=-.Lpc0_%=\n\t"
    "s_addc_u32 s93, s7, 0\n\t"
    "v_mfma_f32_4x4x4_16b_bf16 v[36:39], v[8:9], v[8:9], 0\n\t"
    "v_mfma_f32_4x4x4_16b_bf16 v[40:43], v[8:9], v[8:9], 0\n\t"
    "s_and_b32 s23, s22, 8\n\t"
    "s_and_b32 s24, s22, 7\n\t"
    "s_add_u32 s25, s23, s34\n\t"
    "s_lshl_b32 s23, s23, 3\n\t"
    "s_add_u32 s24, s23, s24\n\t"
    "s_lshl_b32 s25, s25, 7\n\t"
    "s_lshl_b32 s23, s22, 10\n\t"
    "s_sub_u32 s25, s25, s23\n\t"
    "v_lshrrev_b32 v20, 3, v20\n\t"
    "v_add_u32 v47, s25, v15\n\t"
    "s_lshl_b32 s24, s24, 15\n\t"
    "v_and_b32 v47, 0x7fff, v47\n\t"
    "s_add_u32 s24, s24, 32768\n\t"
    "v_lshl_add_u32 v47, v20, 18, v47\n\t"
    "v_mov_b32 v23, 4\n\t"
    "s_mov_b32 s58, 4194111\n\t"
    "s_mov_b32 s27, 0x10000\n\t"
    "s_add_u32 s60, s12, s24\n\t"
    "s_addc_u32 s61, s13, 0\n\t"
    "s_branch .Lgo_%=\n\t"
    ".Lbw_%=:\n\t"
    "v_subrev_u32 v116, 8192, v15\n\t"
    "s_lshr_b32 s23, %[b], 3\n\t"
    "s_add_u32 s23, s23, %[b]\n\t"
    "s_and_b32 s23, s23, 7\n\t"
    "s_andn2_b32 s34, %[b], 7\n\t"
    "s_or_b32 s34, s34, s23\n\t"
    "s_mov_b32 s28, s14\n\t"
    "s_and_b32 s29, s15, 0xffff\n\t"
    "s_mov_b32 s30, 2560000\n\t"
    "s_mov_b32 s31, 0x20000\n\t"
    "v_bfe_u32 v50, %[tid], 4, 5\n\t"
    "s_lshl_b32 s25, s34, 8\n\t"
    "v_xor_b32 v117, v50, %[tid]\n\t"
    "v_lshl_add_u32 v50, v50, 16, s25\n\t"
    "v_and_b32 v117, 15, v117\n\t"
    "v_lshl_add_u32 v117, v117, 4, v50\n\t"
    "v_and_b32 v16, 60, %[tid]\n\t"
    "v_and_b32 v17, 31, %[tid]\n\t"
    "v_lshlrev_b32 v16, 6, v16\n\t"
    "v_and_b32 v19, 48, %[tid]\n\t"
    "v_and_b32 v25, 28, %[tid]\n\t"
    "v_lshl_add_u32 v17, v17, 3, v16\n\t"
    "s_and_b32 s23, s22, 7\n\t"
    "s_lshl_b32 s23, s23, 5\n\t"
    "v_xor_b32 v21, v18, v19\n\t"
    "v_xor_b32 v17, s23, v17\n\t"
    "v_lshl_add_u32 v21, v18, 4, v21\n\t"
    "v_add_u32 v17, 92928, v17\n\t"
    "v_add_u32 v21, 80640, v21\n\t"
    "s_add_u32 s23, s23, 54528\n\t"
    "v_lshrrev_b32 v24, 1, v19\n\t"
    "v_xor_b32 v22, 64, v21\n\t"
    "v_mad_u32_u24 v24, v18, 17, v24\n\t"
    "v_xor_b32 v46, 0x80, v21\n\t"
    "v_xor_b32 v44, 0xc0, v21\n\t"
    "v_add_u32 v45, s23, v24\n\t"
    "v_lshlrev_b32 v25, 3, v25\n\t"
    "v_and_b32 v26, 0x30, v15\n\t"
    "s_and_b32 s23, s22, 3\n\t"
    "s_bfe_u32 s24, s22, 0x10002\n\t"
    "s_lshl_b32 s23, s23, 6\n\t"
    "v_bitop3_b32 v25, v25, v26, s23 bitop3:0x96\n\t"
    "s_mul_i32 s25, s24, 4352\n\t"
    "s_add_u32 s25, s25, 67584\n\t"
    "v_add_u32 v16, 125696, v16\n\t"
    "v_mad_u32_u24 v11, v18, 17, v19\n\t"
    "v_add_u32 v10, v16, v25\n\t"
    "s_lshl_b32 s24, s24, 20\n\t"
    "v_add_u32 v11, s25, v11\n\t"
    "s_add_u32 s24, s24, s23\n\t"
    "s_lshl_b32 s23, s34, 8\n\t"
    "s_add_u32 s24, s24, s23\n\t"
    "v_lshrrev_b32 v18, 2, v18\n\t"
    "v_lshl_add_u32 v19, v19, 14, s24\n\t"
    "v_mov_b32 v13, 0x05040100\n\t"
    "v_add_u32 v12, v19, v18\n\t"
    "s_mov_b32 exec_lo, 0xcccccccc\n\t"
    "s_mov_b32 exec_hi, 0xcccccccc\n\t"
    "v_mov_b32 v13, 0x07060302\n\t"
    "s_mov_b64 exec, -1\n\t"
    "v_and_b32 v16, 0x3f0, v15\n\t"
    "s_add_u32 s26, s26, 92928\n\t"
    "v_add_u32 v16, s26, v16\n\t"
    "s_mov_b32 s88, s20\n\t"
    "s_and_b32 s89, s21, 0xffff\n\t"
    "s_mov_b32 s90, 2560000\n\t"
    "s_mov_b32 s91, 0x20000\n\t"
    "v_and_b32 v20, 63, %[tid]\n\t"
    "s_mul_i32 s23, s22, 272\n\t"
    "v_lshlrev_b32 v14, 2, v20\n\t"
    "s_add_u32 s24, s23, 1280\n\t"
    "s_add_u32 s25, s23, 54528\n\t"
    "s_add_u32 s23, s23, 67584\n\t"
    "v_and_or_b32 v1, v20, 12, s24\n\t"
    "v_bfi_b32 v4, 63, v15, v14\n\t"
    "s_mov_b64 s[4:5], 0xffff\n\t"
    "v_add_u32 v3, s23, v14\n\t"
    "v_add_u32 v2, s25, v4\n\t"
    "v_lshlrev_b64 v[6:7], v15, s[4:5]\n\t"
    "v_mov_b32 v5, 1\n\t"
    "s_mov_b32 s48, 0x000f000f\n\t"
    "s_mov_b32 s49, 0x000f000f\n\t"
    "s_mov_b32 s94, 0x22222222\n\t"
    "s_mov_b32 s95, 0x22222222\n\t"
    "s_lshl_b64 s[96:97], s[94:95], 1\n\t"
    "s_mov_b32 s57, 0\n\t"
    "s_mov_b32 s55, 1\n\t"
    "s_getpc_b64 s[6:7]\n\t"
    ".Lpc1_%=:\n\t"
    "s_add_u32 s8, s6, .Lsend_%=-.Lpc1_%=\n\t"
    "s_addc_u32 s9, s7, 0\n\t"
    "s_add_u32 s10, s6, .Laend_%=-.Lpc1_%=\n\t"
    "s_addc_u32 s11, s7, 0\n\t"
    "s_add_u32 s92, s6, .Llend_%=-.Lpc1_%=\n\t"
    "s_addc_u32 s93, s7, 0\n\t"
    "v_mfma_f32_4x4x4_16b_bf16 v[36:39], v[8:9], v[8:9], 0\n\t"
    "v_mfma_f32_4x4x4_16b_bf16 v[40:43], v[8:9], v[8:9], 0\n\t"
    "s_and_b32 s23, s22, 8\n\t"
    "s_and_b32 s24, s22, 7\n\t"
    "s_add_u32 s25, s23, s34\n\t"
    "s_lshl_b32 s23, s23, 3\n\t"
    "s_add_u32 s24, s23, s24\n\t"
    "s_lshl_b32 s25, s25, 7\n\t"
    "s_lshl_b32 s23, s22, 10\n\t"
    "s_sub_u32 s25, s25, s23\n\t"
    "v_lshrrev_b32 v20, 3, v20\n\t"
    "v_add_u32 v47, s25, v15\n\t"
    "s_lshl_b32 s24, s24, 15\n\t"
    "v_and_b32 v47, 0x7fff, v47\n\t"
    "s_add_u32 s24, s24, 32768\n\t"
    "v_lshl_add_u32 v47, v20, 18, v47\n\t"
    "v_mov_b32 v23, 4\n\t"
    "s_mov_b32 s58, 4194111\n\t"
    "s_mov_b32 s27, 0x10000\n\t"
    "s_add_u32 s60, s12, s24\n\t"
    "s_addc_u32 s61, s13, 0\n\t"
    "s_waitcnt lgkmcnt(0)\n\t"
    "s_add_u32 s36, s36, 0x01400000\n\t"
    "s_and_b32 s36, s36, s59\n\t"
    "s_add_u32 s37, s37, 0x01400000\n\t"
    "s_and_b32 s37, s37, s59\n\t"
    "s_add_u32 s38, s38, 0x01400000\n\t"
    "s_and_b32 s38, s38, s59\n\t"
    "s_add_u32 s39, s39, 0x01400000\n\t"
    "s_and_b32 s39, s39, s59\n\t"
    "s_cmp_ge_u32 %[b], 128\n\t"
    "s_cbranch_scc1 .Lbs_%=\n\t"
    ".Lspc_%=:\n\t"
    "ds_read_b32 v24, v8 offset:1268\n\t"
    "s_waitcnt lgkmcnt(0)\n\t"
    "v_cmp_gt_u32 vcc, 32, v24\n\t"
    "s_cbranch_vccnz .Lspc_%=\n\t"
    "s_sleep 2\n\t"
    ".Lbs_%=:\n\t"
    "ds_write_b128 v116, v[112:115] offset:1280\n\t"
    "ds_write_b128 v116, v[112:115] offset:5376\n\t"
    "s_lshl_b32 s23, s22, 10\n\t"
    "s_add_u32 m0, s23, 72448\n\t"
    "s_cmp_ge_u32 s22, 10\n\t"
    "s_waitcnt vmcnt(0)\n\t"
    "ds_write_b128 v16, v[64:67] offset:0\n\t"
    "ds_write_b128 v16, v[68:71] offset:4096\n\t"
    "ds_write_b128 v16, v[72:75] offset:8192\n\t"
    "ds_write_b128 v16, v[76:79] offset:12288\n\t"
    "s_waitcnt lgkmcnt(0)\n\t"
    "s_barrier\n\t"
    "buffer_load_dwordx4 v117, s[28:31], 0 offen lds\n\t"
    "s_cbranch_scc1 .Lxo1_%=\n\t"
    "s_add_u32 m0, m0, 0x2000\n\t"
    "v_add_u32 v117, 0x200000, v117\n\t"
    "buffer_load_dwordx4 v117, s[28:31], 0 offen lds\n\t"
    ".Lxo1_%=:\n\t"
    "ds_read_b64_tr_b16 v[48:49], v17 offset:0\n\t"
    "ds_read_b64_tr_b16 v[50:51], v17 offset:4096\n\t"
    "ds_read_b64_tr_b16 v[52:53], v17 offset:8192\n\t"
    "ds_read_b64_tr_b16 v[54:55], v17 offset:12288\n\t"
    "ds_read_b64_tr_b16 v[56:57], v17 offset:16384\n\t"
    "ds_read_b64_tr_b16 v[58:59], v17 offset:20480\n\t"
    "ds_read_b64_tr_b16 v[60:61], v17 offset:24576\n\t"
    "ds_read_b64_tr_b16 v[62:63], v17 offset:28672\n\t"
    ".Lgo_%=:\n\t"
    "s_cmp_ge_u32 %[b], 128\n\t"
    "s_movk_i32 s24, 190\n\t"
    "s_cselect_b32 s24, 190, s24\n\t"
    ".Lgate_%=:\n\t"
    "s_memrealtime s[62:63]\n\t"
    "s_waitcnt lgkmcnt(0)\n\t"
    "s_sub_u32 s23, s62, s98\n\t"
    "s_cmp_lt_u32 s23, s24\n\t"
    "s_cbranch_scc1 .Lgate_%=\n\t"
    "s_lshr_b32 s23, %[b], 3\n\t"
    "s_mul_i32 s23, s23, 16384\n\t"
    "s_add_u32 s23, s23, 393216\n\t"
    "v_lshl_add_u32 v16, %[tid], 7, s23\n\t"
    "global_load_dwordx4 v[64:67], v47, s[60:61] sc1\n\t"
    "s_cmp_lt_u32 s22, 8\n\t"
    "s_cbranch_scc1 .Lq9_%=\n\t"
    "s_waitcnt vmcnt(1)\n\t"
    "s_mov_b64 exec, 1\n\t"
    "ds_add_u32 v8, v5 offset:1264\n\t"
    "s_mov_b64 exec, -1\n\t"
    ".Lspx_%=:\n\t"
    "ds_read_b32 v24, v8 offset:1264\n\t"
    "s_waitcnt lgkmcnt(0)\n\t"
    "v_cmp_gt_u32 vcc, 8, v24\n\t"
    "s_cbranch_vccnz .Lspx_%=\n\t"
    "ds_read_b128 v[68:71], v21 offset:0\n\t"
    "ds_read_b128 v[72:75], v21 offset:4096\n\t"
    "ds_read_b128 v[76:79], v21 offset:8192\n\t"
    "ds_read_b128 v[80:83], v22 offset:0\n\t"
    "ds_read_b128 v[84:87], v22 offset:4096\n\t"
    "ds_read_b128 v[88:91], v22 offset:8192\n\t"
    "ds_read_b128 v[92:95], v46 offset:0\n\t"
    "ds_read_b128 v[96:99], v46 offset:4096\n\t"
    "ds_read_b128 v[100:103], v46 offset:8192\n\t"
    "ds_read_b128 v[104:107], v44 offset:0\n\t"
    "ds_read_b128 v[108:111], v44 offset:4096\n\t"
    "ds_read_b128 v[112:115], v44 offset:8192\n\t"
    "s_waitcnt lgkmcnt(9)\n\t"
    "v_mfma_f32_16x16x32_bf16 v[24:27], v[48:51], v[68:71], 0\n\t"
    "v_mfma_f32_16x16x32_bf16 v[28:31], v[48:51], v[72:75], 0\n\t"
    "v_mfma_f32_16x16x32_bf16 v[32:35], v[48:51], v[76:79], 0\n\t"
    "s_waitcnt lgkmcnt(6)\n\t"
    "v_mfma_f32_16x16x32_bf16 v[24:27], v[52:55], v[80:83], v[24:27]\n\t"
    "v_mfma_f32_16x16x32_bf16 v[28:31], v[52:55], v[84:87], v[28:31]\n\t"
    "v_mfma_f32_16x16x32_bf16 v[32:35], v[52:55], v[88:91], v[32:35]\n\t"
    "s_waitcnt lgkmcnt(3)\n\t"
    "v_mfma_f32_16x16x32_bf16 v[24:27], v[56:59], v[92:95], v[24:27]\n\t"
    "v_mfma_f32_16x16x32_bf16 v[28:31], v[56:59], v[96:99], v[28:31]\n\t"
    "v_mfma_f32_16x16x32_bf16 v[32:35], v[56:59], v[100:103], v[32:35]\n\t"
    "s_waitcnt lgkmcnt(0)\n\t"
    "v_mfma_f32_16x16x32_bf16 v[24:27], v[60:63], v[104:107], v[24:27]\n\t"
    "v_mfma_f32_16x16x32_bf16 v[28:31], v[60:63], v[108:111], v[28:31]\n\t"
    "v_mfma_f32_16x16x32_bf16 v[32:35], v[60:63], v[112:115], v[32:35]\n\t"
    "s_nop 5\n\t"
    "v_cvt_pk_bf16_f32 v24, v24, v25\n\t"
    "v_cvt_pk_bf16_f32 v25, v26, v27\n\t"
    "ds_write_b64 v45, v[24:25] offset:0\n\t"
    "v_cvt_pk_bf16_f32 v28, v28, v29\n\t"
    "v_cvt_pk_bf16_f32 v29, v30, v31\n\t"
    "ds_write_b64 v45, v[28:29] offset:4352\n\t"
    "v_cvt_pk_bf16_f32 v32, v32, v33\n\t"
    "v_cvt_pk_bf16_f32 v33, v34, v35\n\t"
    "ds_write_b64 v45, v[32:33] offset:8704\n\t"
    ".Lq9_%=:\n\t"
    "v_mfma_f32_32x32x2_f32 v[48:63], v8, v8, 0\n\t"
    "s_branch .Lpoll1_%=\n\t"
    ".Ltouch0_%=:\n\t"
    "s_branch .Lbw_%=\n\t"
    ".Lpoll_%=:\n\t"
    "s_sleep 4\n\t"
    "global_load_dwordx4 v[64:67], v47, s[60:61] sc1\n\t"
    ".Lpoll1_%=:\n\t"
    "s_sub_u32 s27, s27, 1\n\t"
    "s_cbranch_scc1 .Lbail_%=\n\t"
    "s_waitcnt vmcnt(0)\n\t"
    "v_xor_b32 v64, s36, v64\n\t"
    "v_xor_b32 v65, s37, v65\n\t"
    "v_xor_b32 v66, s38, v66\n\t"
    "v_xor_b32 v67, s39, v67\n\t"
    "v_or3_b32 v19, v64, v65, v66\n\t"
    "v_bitop3_b32 v19, v19, s59, v67 bitop3:0xc8\n\t"
    "v_cmpx_ne_u32 vcc, 0, v19\n\t"
    "s_cbranch_vccnz .Lpoll_%=\n\t"
    "s_mov_b64 exec, -1\n\t"
    "s_cmp_ge_u32 s22, 2\n\t"
    "s_cbranch_scc1 .Lpf3_%=\n\t"
    "global_load_dword v126, v16, s[14:15]\n\t"
    ".Lpf3_%=:\n\t"
    "v_cmp_ne_u32 s[40:41], s58, v64\n\t"
    "v_lshlrev_b32_sdwa v72, 2, v64 dst_sel:DWORD dst_unused:UNUSED_PAD src0_sel:DWORD src1_sel:BYTE_0\n\t"
    "v_cmp_ne_u32 s[42:43], s58, v65\n\t"
    "v_lshlrev_b32_sdwa v73, 2, v65 dst_sel:DWORD dst_unused:UNUSED_PAD src0_sel:DWORD src1_sel:BYTE_0\n\t"
    "v_cmp_ne_u32 s[44:45], s58, v66\n\t"
    "v_lshlrev_b32_sdwa v74, 2, v66 dst_sel:DWORD dst_unused:UNUSED_PAD src0_sel:DWORD src1_sel:BYTE_0\n\t"
    "s_mov_b64 exec, s[40:41]\n\t"
    "ds_add_rtn_u32 v80, v72, v23 offset:1024\n\t"
    "s_mov_b64 exec, s[42:43]\n\t"
    "ds_add_rtn_u32 v81, v73, v23 offset:1024\n\t"
    "s_mov_b64 exec, s[44:45]\n\t"
    "ds_add_rtn_u32 v82, v74, v23 offset:1024\n\t"
    "s_mov_b64 exec, -1\n\t"
    "s_mov_b32 s23, 68\n\t"
    "v_cmp_ne_u32 vcc, s58, v67\n\t"
    "v_and_b32 v64, 0xffffff00, v64\n\t"
    "v_and_b32 v65, 0xffffff00, v65\n\t"
    "v_and_b32 v66, 0xffffff00, v66\n\t"
    "s_cbranch_vccnz .Lins3_%=\n\t"
    ".Lins3b_%=:\n\t"
    "s_waitcnt lgkmcnt(0)\n\t"
    "s_mov_b64 exec, s[40:41]\n\t"
    "v_min_u32 v80, 0x100, v80\n\t"
    "v_mad_u32_u24 v80, v72, s23, v80\n\t"
    "ds_write_b32 v80, v64 offset:1280\n\t"
    "s_mov_b64 exec, s[42:43]\n\t"
    "v_min_u32 v81, 0x100, v81\n\t"
    "v_mad_u32_u24 v81, v73, s23, v81\n\t"
    "ds_write_b32 v81, v65 offset:1280\n\t"
    "s_mov_b64 exec, s[44:45]\n\t"
    "v_min_u32 v82, 0x100, v82\n\t"
    "v_mad_u32_u24 v82, v74, s23, v82\n\t"
    "ds_write_b32 v82, v66 offset:1280\n\t"
    "s_mov_b64 exec, -1\n\t"
    ".Lins_done_%=:\n\t"
    "s_add_u32 s24, s22, 16\n\t"
    "s_add_u32 s25, s22, 32\n\t"
    "s_sub_u32 s26, 10255, s34\n\t"
    "s_lshr_b32 s26, s26, 8\n\t"
    "s_setprio 2\n\t"
    "s_waitcnt lgkmcnt(0)\n\t"
    "s_barrier\n\t"
    "s_cmp_eq_u32 s22, 15\n\t"
    "s_cbranch_scc1 .Lackst_%=\n\t"
    ".Lackstb_%=:\n\t"
    "ds_read_b32 v16, v14 offset:1024\n\t"
    "ds_read2_b32 v[34:35], v1 offset0:40 offset1:44\n\t"
    "ds_read2_b32 v[32:33], v1 offset0:32 offset1:36\n\t"
    "ds_read2_b32 v[30:31], v1 offset0:24 offset1:28\n\t"
    "ds_read2_b32 v[28:29], v1 offset0:16 offset1:20\n\t"
    "ds_read2_b32 v[26:27], v1 offset0:8 offset1:12\n\t"
    "ds_read2_b32 v[24:25], v1 offset0:0 offset1:4\n\t"
    "s_cmp_lt_u32 s25, s26\n\t"
    "s_waitcnt lgkmcnt(6)\n\t"
    "v_cmp_lt_u32 vcc, 0x100, v16\n\t"
    "v_add_u32 v16, 12, v16\n\t"
    "v_lshrrev_b32 v16, 4, v16\n\t"
    "v_max_u32 v16, 4, v16\n\t"
    "s_cbranch_vccnz .Lslow_%=\n\t"
    "v_readlane_b32 s52, v16, s25\n\t"
    "v_readlane_b32 s50, v16, s22\n\t"
    "v_readlane_b32 s51, v16, s24\n\t"
    "s_cselect_b32 s52, s52, 0\n\t"
    "s_min_u32 s54, s50, 12\n\t"
    "s_sub_u32 s56, s50, s54\n\t"
    "s_cmp_lg_u32 s56, 0\n\t"
    "s_cselect_b32 s50, 4, s51\n\t"
    "s_cselect_b32 s51, s51, s52\n\t"
    "s_cselect_b32 s52, s52, 0\n\t"
    "s_mul_i32 s23, s54, 12\n\t"
    "s_sub_u32 s6, s92, s23\n\t"
    "s_subb_u32 s7, s93, 0\n\t"
    "s_waitcnt lgkmcnt(0)\n\t"
    "s_setpc_b64 s[6:7]\n\t"
    ".Ltouch1_%=:\n\t"
    "s_branch .Lbw_%=\n\t"
    "s_waitcnt vmcnt(11)\n\t"
    "v_mfma_f32_16x16x32_bf16 v[32:35], v[60:63], v[108:111], 0\n\t"
    "s_waitcnt vmcnt(10)\n\t"
    "v_mfma_f32_16x16x32_bf16 v[32:35], v[56:59], v[104:107], v[32:35]\n\t"
    "s_waitcnt vmcnt(9)\n\t"
    "v_mfma_f32_16x16x32_bf16 v[32:35], v[52:55], v[100:103], v[32:35]\n\t"
    "s_waitcnt vmcnt(8)\n\t"
    "v_mfma_f32_16x16x32_bf16 v[32:35], v[48:51], v[96:99], v[32:35]\n\t"
    "s_waitcnt vmcnt(7)\n\t"
    "v_mfma_f32_16x16x32_bf16 v[28:31], v[60:63], v[92:95], 0\n\t"
    "s_waitcnt vmcnt(6)\n\t"
    "v_mfma_f32_16x16x32_bf16 v[28:31], v[56:59], v[88:91], v[28:31]\n\t"
    "s_waitcnt vmcnt(5)\n\t"
    "v_mfma_f32_16x16x32_bf16 v[28:31], v[52:55], v[84:87], v[28:31]\n\t"
    "s_waitcnt vmcnt(4)\n\t"
    "v_mfma_f32_16x16x32_bf16 v[28:31], v[48:51], v[80:83], v[28:31]\n\t"
    "s_waitcnt vmcnt(3)\n\t"
    "v_mfma_f32_16x16x32_bf16 v[24:27], v[60:63], v[76:79], 0\n\t"
    "s_waitcnt vmcnt(2)\n\t"
    "v_mfma_f32_16x16x32_bf16 v[24:27], v[56:59], v[72:75], v[24:27]\n\t"
    "s_waitcnt vmcnt(1)\n\t"
    "v_mfma_f32_16x16x32_bf16 v[24:27], v[52:55], v[68:71], v[24:27]\n\t"
    "s_waitcnt vmcnt(0)\n\t"
    "v_mfma_f32_16x16x32_bf16 v[24:27], v[48:51], v[64:67], v[24:27]\n\t"
    ".Lsend_%=:\n\t"
    "s_min_u32 s54, s50, 12\n\t"
    "s_sub_u32 s56, s50, s54\n\t"
    "s_cmp_lg_u32 s56, 0\n\t"
    "s_cselect_b32 s50, 4, s51\n\t"
    "s_cselect_b32 s51, s51, s52\n\t"
    "s_cselect_b32 s52, s52, 0\n\t"
    "s_mul_i32 s23, s53, 20\n\t"
    "s_sub_u32 s6, s10, s23\n\t"
    "s_subb_u32 s7, s11, 0\n\t"
    "s_cmp_eq_u32 s54, 0\n\t"
    "v_add_f32_dpp v20, v32, v34 quad_perm:[0,0,0,0] row_mask:0xf bank_mask:0xf\n\t"
    "v_add_f32_dpp v18, v28, v29 quad_perm:[0,0,0,0] row_mask:0xf bank_mask:0xf\n\t"
    "v_add_f32_dpp v16, v25, v24 quad_perm:[1,1,1,1] row_mask:0xf bank_mask:0xf\n\t"
    "v_add_f32_dpp v20, v33, v20 quad_perm:[1,1,1,1] row_mask:0xf bank_mask:0xf\n\t"
    "v_add_f32_dpp v18, v30, v18 quad_perm:[2,2,2,2] row_mask:0xf bank_mask:0xf\n\t"
    "v_add_f32_dpp v16, v26, v16 quad_perm:[2,2,2,2] row_mask:0xf bank_mask:0xf\n\t"
    "v_add_f32_dpp v20, v35, v20 quad_perm:[3,3,3,3] row_mask:0xf bank_mask:0xf\n\t"
    "v_add_f32_dpp v18, v31, v18 quad_perm:[3,3,3,3] row_mask:0xf bank_mask:0xf\n\t"
    "v_add_f32_dpp v16, v27, v16 quad_perm:[3,3,3,3] row_mask:0xf bank_mask:0xf\n\t"
    "s_cbranch_scc1 .Lnonext_%=\n\t"
    "s_cmp_lg_u32 s55, 0\n\t"
    "s_cbranch_scc1 .Lcont_%=\n\t"
    "v_add_u32 v1, 4352, v1\n\t"
    ".Lcontb_%=:\n\t"
    "ds_read2_b32 v[34:35], v1 offset0:40 offset1:44\n\t"
    "ds_read2_b32 v[32:33], v1 offset0:32 offset1:36\n\t"
    "ds_read2_b32 v[30:31], v1 offset0:24 offset1:28\n\t"
    "ds_read2_b32 v[28:29], v1 offset0:16 offset1:20\n\t"
    "ds_read2_b32 v[26:27], v1 offset0:8 offset1:12\n\t"
    "ds_read2_b32 v[24:25], v1 offset0:0 offset1:4\n\t"
    ".Lnonext_%=:\n\t"
    "v_cndmask_b32 v22, v16, v18, s[94:95]\n\t"
    "v_cndmask_b32 v22, v22, v20, s[96:97]\n\t"
    "v_mul_f32 v22, 0xbe0293ee, v22\n\t"
    "v_exp_f32 v22, v22\n\t"
    "s_nop 0\n\t"
    "v_add_f32 v22, 1.0, v22\n\t"
    "v_rcp_f32 v22, v22\n\t"
    "s_nop 0\n\t"
    "v_cvt_pk_bf16_f32 v22, v22, v22\n\t"
    "s_cmp_eq_u32 s54, 0\n\t"
    "s_nop 0\n\t"
    "v_and_b32_dpp v20, v22, v6 quad_perm:[2,2,2,2] row_mask:0xf bank_mask:0xf\n\t"
    "v_and_b32_dpp v21, v22, v7 quad_perm:[2,2,2,2] row_mask:0xf bank_mask:0xf\n\t"
    "v_and_b32_dpp v18, v22, v6 quad_perm:[1,1,1,1] row_mask:0xf bank_mask:0xf\n\t"
    "v_and_b32_dpp v19, v22, v7 quad_perm:[1,1,1,1] row_mask:0xf bank_mask:0xf\n\t"
    "v_and_b32_dpp v16, v22, v6 quad_perm:[0,0,0,0] row_mask:0xf bank_mask:0xf\n\t"
    "v_and_b32_dpp v17, v22, v7 quad_perm:[0,0,0,0] row_mask:0xf bank_mask:0xf\n\t"
    "s_setpc_b64 s[6:7]\n\t"
    ".Ltouch2_%=:\n\t"
    "s_branch .Lbw_%=\n\t"
    "s_nop 0\n\t"
    "v_mfma_f32_4x4x4_16b_bf16 v[36:39], v[108:109], v[20:21], v[36:39] blgp:7\n\t"
    "v_mfma_f32_4x4x4_16b_bf16 v[40:43], v[110:111], v[20:21], v[40:43] blgp:7\n\t"
    "s_nop 0\n\t"
    "v_mfma_f32_4x4x4_16b_bf16 v[36:39], v[104:105], v[20:21], v[36:39] blgp:6\n\t"
    "v_mfma_f32_4x4x4_16b_bf16 v[40:43], v[106:107], v[20:21], v[40:43] blgp:6\n\t"
    "s_nop 0\n\t"
    "v_mfma_f32_4x4x4_16b_bf16 v[36:39], v[100:101], v[20:21], v[36:39] blgp:5\n\t"
    "v_mfma_f32_4x4x4_16b_bf16 v[40:43], v[102:103], v[20:21], v[40:43] blgp:5\n\t"
    "s_nop 0\n\t"
    "v_mfma_f32_4x4x4_16b_bf16 v[36:39], v[96:97], v[20:21], v[36:39] blgp:4\n\t"
    "v_mfma_f32_4x4x4_16b_bf16 v[40:43], v[98:99], v[20:21], v[40:43] blgp:4\n\t"
    "s_nop 0\n\t"
    "v_mfma_f32_4x4x4_16b_bf16 v[36:39], v[92:93], v[18:19], v[36:39] blgp:7\n\t"
    "v_mfma_f32_4x4x4_16b_bf16 v[40:43], v[94:95], v[18:19], v[40:43] blgp:7\n\t"
    "s_nop 0\n\t"
    "v_mfma_f32_4x4x4_16b_bf16 v[36:39], v[88:89], v[18:19], v[36:39] blgp:6\n\t"
    "v_mfma_f32_4x4x4_16b_bf16 v[40:43], v[90:91], v[18:19], v[40:43] blgp:6\n\t"
    "s_nop 0\n\t"
    "v_mfma_f32_4x4x4_16b_bf16 v[36:39], v[84:85], v[18:19], v[36:39] blgp:5\n\t"
    "v_mfma_f32_4x4x4_16b_bf16 v[40:43], v[86:87], v[18:19], v[40:43] blgp:5\n\t"
    "s_nop 0\n\t"
    "v_mfma_f32_4x4x4_16b_bf16 v[36:39], v[80:81], v[18:19], v[36:39] blgp:4\n\t"
    "v_mfma_f32_4x4x4_16b_bf16 v[40:43], v[82:83], v[18:19], v[40:43] blgp:4\n\t"
    "s_nop 0\n\t"
    "v_mfma_f32_4x4x4_16b_bf16 v[36:39], v[76:77], v[16:17], v[36:39] blgp:7\n\t"
    "v_mfma_f32_4x4x4_16b_bf16 v[40:43], v[78:79], v[16:17], v[40:43] blgp:7\n\t"
    "s_nop 0\n\t"
    "v_mfma_f32_4x4x4_16b_bf16 v[36:39], v[72:73], v[16:17], v[36:39] blgp:6\n\t"
    "v_mfma_f32_4x4x4_16b_bf16 v[40:43], v[74:75], v[16:17], v[40:43] blgp:6\n\t"
    "s_nop 0\n\t"
    "v_mfma_f32_4x4x4_16b_bf16 v[36:39], v[68:69], v[16:17], v[36:39] blgp:5\n\t"
    "v_mfma_f32_4x4x4_16b_bf16 v[40:43], v[70:71], v[16:17], v[40:43] blgp:5\n\t"
    "s_nop 0\n\t"
    "v_mfma_f32_4x4x4_16b_bf16 v[36:39], v[64:65], v[16:17], v[36:39] blgp:4\n\t"
    "v_mfma_f32_4x4x4_16b_bf16 v[40:43], v[66:67], v[16:17], v[40:43] blgp:4\n\t"
    ".Laend_%=:\n\t"
    "s_cbranch_scc1 .Llend_%=\n\t"
    "s_setprio 1\n\t"
    "s_cmp_lt_u32 s57, 2\n\t"
    "s_cbranch_scc1 .Lpr1_%=\n\t"
    "s_setprio 0\n\t"
    ".Lpr1_%=:\n\t"
    "s_mul_i32 s23, s54, 12\n\t"
    "s_sub_u32 s6, s92, s23\n\t"
    "s_subb_u32 s7, s93, 0\n\t"
    "s_waitcnt lgkmcnt(0)\n\t"
    "s_setpc_b64 s[6:7]\n\t"
    ".Ltouch3_%=:\n\t"
    "s_branch .Lbw_%=\n\t"
    "v_add_u32 v35, v35, v4\n\t"
    "buffer_load_dwordx4 v[108:111], v35, s[28:31], 0 offen\n\t"
    "v_add_u32 v34, v34, v4\n\t"
    "buffer_load_dwordx4 v[104:107], v34, s[28:31], 0 offen\n\t"
    "v_add_u32 v33, v33, v4\n\t"
    "buffer_load_dwordx4 v[100:103], v33, s[28:31], 0 offen\n\t"
    "v_add_u32 v32, v32, v4\n\t"
    "buffer_load_dwordx4 v[96:99], v32, s[28:31], 0 offen\n\t"
    "v_add_u32 v31, v31, v4\n\t"
    "buffer_load_dwordx4 v[92:95], v31, s[28:31], 0 offen\n\t"
    "v_add_u32 v30, v30, v4\n\t"
    "buffer_load_dwordx4 v[88:91], v30, s[28:31], 0 offen\n\t"
    "v_add_u32 v29, v29, v4\n\t"
    "buffer_load_dwordx4 v[84:87], v29, s[28:31], 0 offen\n\t"
    "v_add_u32 v28, v28, v4\n\t"
    "buffer_load_dwordx4 v[80:83], v28, s[28:31], 0 offen\n\t"
    "v_add_u32 v27, v27, v4\n\t"
    "buffer_load_dwordx4 v[76:79], v27, s[28:31], 0 offen\n\t"
    "v_add_u32 v26, v26, v4\n\t"
    "buffer_load_dwordx4 v[72:75], v26, s[28:31], 0 offen\n\t"
    "v_add_u32 v25, v25, v4\n\t"
    "buffer_load_dwordx4 v[68:71], v25, s[28:31], 0 offen\n\t"
    "v_add_u32 v24, v24, v4\n\t"
    "buffer_load_dwordx4 v[64:67], v24, s[28:31], 0 offen\n\t"
    ".Llend_%=:\n\t"
    "s_cmp_lg_u32 s55, 0\n\t"
    "s_cbranch_scc1 .Lnoepi_%=\n\t"
    "s_cmp_lt_u32 s57, 3\n\t"
    "s_cselect_b64 s[4:5], 1, 0\n\t"
    "v_add_f32_dpp v36, v36, v36 row_ror:8 row_mask:0xf bank_mask:0x3\n\t"
    "v_add_f32_dpp v37, v37, v37 row_ror:8 row_mask:0xf bank_mask:0x3\n\t"
    "v_add_f32_dpp v40, v40, v40 row_ror:8 row_mask:0xf bank_mask:0x3\n\t"
    "v_add_f32_dpp v41, v41, v41 row_ror:8 row_mask:0xf bank_mask:0x3\n\t"
    "v_add_f32_dpp v36, v38, v38 row_ror:8 row_mask:0xf bank_mask:0xc\n\t"
    "v_add_f32_dpp v37, v39, v39 row_ror:8 row_mask:0xf bank_mask:0xc\n\t"
    "v_add_f32_dpp v40, v42, v42 row_ror:8 row_mask:0xf bank_mask:0xc\n\t"
    "v_add_f32_dpp v41, v43, v43 row_ror:8 row_mask:0xf bank_mask:0xc\n\t"
    "v_add_f32_dpp v36, v36, v36 row_shl:4 row_mask:0xf bank_mask:0x5\n\t"
    "v_add_f32_dpp v40, v40, v40 row_shl:4 row_mask:0xf bank_mask:0x5\n\t"
    "s_cmp_eq_u32 s54, 0\n\t"
    "v_add_f32_dpp v36, v37, v37 row_shr:4 row_mask:0xf bank_mask:0xa\n\t"
    "v_add_f32_dpp v40, v41, v41 row_shr:4 row_mask:0xf bank_mask:0xa\n\t"
    "v_cvt_pk_bf16_f32 v22, v36, v40\n\t"
    "ds_write_b32 v3, v22\n\t"
    "s_mov_b64 exec, s[4:5]\n\t"
    "ds_add_u32 v8, v5 offset:1272\n\t"
    "s_mov_b64 exec, -1\n\t"
    "s_cbranch_scc1 .Ldone_%=\n\t"
    "v_mfma_f32_4x4x4_16b_bf16 v[36:39], v[8:9], v[8:9], 0\n\t"
    "v_mfma_f32_4x4x4_16b_bf16 v[40:43], v[8:9], v[8:9], 0\n\t"
    "v_add_u32 v3, 4352, v3\n\t"
    "v_add_u32 v2, 4352, v2\n\t"
    ".Lfirst_%=:\n\t"
    "s_add_u32 s57, s57, 1\n\t"
    "s_mov_b64 exec, s[48:49]\n\t"
    "ds_read_b128 v[48:51], v2\n\t"
    "s_lshl_b64 exec, exec, 4\n\t"
    "ds_read_b128 v[52:55], v2\n\t"
    "s_lshl_b64 exec, exec, 4\n\t"
    "ds_read_b128 v[56:59], v2\n\t"
    "s_lshl_b64 exec, exec, 4\n\t"
    "ds_read_b128 v[60:63], v2\n\t"
    "s_mov_b64 exec, -1\n\t"
    ".Lnoepib_%=:\n\t"
    "v_mfma_f32_4x4x4_16b_bf16 v[32:35], v[8:9], v[8:9], 0\n\t"
    "v_mfma_f32_4x4x4_16b_bf16 v[28:31], v[8:9], v[8:9], 0\n\t"
    "s_mov_b32 s53, s54\n\t"
    "s_mov_b32 s55, s56\n\t"
    "s_mul_i32 s23, s53, 12\n\t"
    "s_sub_u32 s6, s8, s23\n\t"
    "s_subb_u32 s7, s9, 0\n\t"
    "s_waitcnt lgkmcnt(0)\n\t"
    "s_setpc_b64 s[6:7]\n\t"
    ".Ltouch4_%=:\n\t"
    "s_branch .Lbw_%=\n\t"
    ".Lnoepi_%=:\n\t"
    "s_cmp_eq_u32 s57, 0\n\t"
    "s_cbranch_scc1 .Lfirst_%=\n\t"
    "v_subrev_u32 v1, 192, v1\n\t"
    "s_branch .Lnoepib_%=\n\t"
    ".Ldone_%=:\n\t"
    "s_cmp_lt_u32 s22, 8\n\t"
    "s_cbranch_scc1 .Lafin_%=\n\t"
    "s_setprio 0\n\t"
    ".Lspr_%=:\n\t"
    "ds_read_b32 v23, v8 offset:1272\n\t"
    "s_waitcnt lgkmcnt(0)\n\t"
    "v_cmp_gt_u32 vcc, 32, v23\n\t"
    "s_cbranch_vccnz .Lspr_%=\n\t"
    "ds_read_b64_tr_b16 v[64:65], v10 offset:0\n\t"
    "ds_read_b64_tr_b16 v[66:67], v10 offset:4096\n\t"
    "ds_read_b64_tr_b16 v[80:81], v10 offset:8\n\t"
    "ds_read_b64_tr_b16 v[82:83], v10 offset:4104\n\t"
    "ds_read_b64_tr_b16 v[68:69], v10 offset:8192\n\t"
    "ds_read_b64_tr_b16 v[70:71], v10 offset:12288\n\t"
    "ds_read_b64_tr_b16 v[84:85], v10 offset:8200\n\t"
    "ds_read_b64_tr_b16 v[86:87], v10 offset:12296\n\t"
    "ds_read_b64_tr_b16 v[72:73], v10 offset:16384\n\t"
    "ds_read_b64_tr_b16 v[74:75], v10 offset:20480\n\t"
    "ds_read_b64_tr_b16 v[88:89], v10 offset:16392\n\t"
    "ds_read_b64_tr_b16 v[90:91], v10 offset:20488\n\t"
    "ds_read_b64_tr_b16 v[76:77], v10 offset:24576\n\t"
    "ds_read_b64_tr_b16 v[78:79], v10 offset:28672\n\t"
    "ds_read_b64_tr_b16 v[92:93], v10 offset:24584\n\t"
    "ds_read_b64_tr_b16 v[94:95], v10 offset:28680\n\t"
    "s_mov_b32 s57, 0\n\t"
    ".Ltile_%=:\n\t"
    "ds_read_b128 v[96:99], v11 offset:0\n\t"
    "ds_read_b128 v[100:103], v11 offset:64\n\t"
    "ds_read_b128 v[104:107], v11 offset:128\n\t"
    "ds_read_b128 v[108:111], v11 offset:192\n\t"
    "s_waitcnt lgkmcnt(3)\n\t"
    "v_mfma_f32_16x16x32_bf16 v[24:27], v[96:99], v[64:67], 0\n\t"
    "v_mfma_f32_16x16x32_bf16 v[28:31], v[96:99], v[80:83], 0\n\t"
    "s_waitcnt lgkmcnt(2)\n\t"
    "v_mfma_f32_16x16x32_bf16 v[24:27], v[100:103], v[68:71], v[24:27]\n\t"
    "v_mfma_f32_16x16x32_bf16 v[28:31], v[100:103], v[84:87], v[28:31]\n\t"
    "s_waitcnt lgkmcnt(1)\n\t"
    "v_mfma_f32_16x16x32_bf16 v[24:27], v[104:107], v[72:75], v[24:27]\n\t"
    "v_mfma_f32_16x16x32_bf16 v[28:31], v[104:107], v[88:91], v[28:31]\n\t"
    "s_waitcnt lgkmcnt(0)\n\t"
    "v_mfma_f32_16x16x32_bf16 v[24:27], v[108:111], v[76:79], v[24:27]\n\t"
    "v_mfma_f32_16x16x32_bf16 v[28:31], v[108:111], v[92:95], v[28:31]\n\t"
    "v_add_u32 v17, 65536, v12\n\t"
    "v_add_u32 v18, 131072, v12\n\t"
    "v_add_u32 v19, 196608, v12\n\t"
    "s_nop 4\n\t"
    "v_cvt_pk_bf16_f32 v32, v24, v28\n\t"
    "v_cvt_pk_bf16_f32 v33, v25, v29\n\t"
    "v_cvt_pk_bf16_f32 v34, v26, v30\n\t"
    "v_cvt_pk_bf16_f32 v35, v27, v31\n\t"
    "v_mov_b32_dpp v36, v32 quad_perm:[1,3,1,3] row_mask:0xf bank_mask:0xf\n\t"
    "v_mov_b32_dpp v37, v33 quad_perm:[1,3,1,3] row_mask:0xf bank_mask:0xf\n\t"
    "v_mov_b32_dpp v38, v34 quad_perm:[1,3,1,3] row_mask:0xf bank_mask:0xf\n\t"
    "v_mov_b32_dpp v39, v35 quad_perm:[1,3,1,3] row_mask:0xf bank_mask:0xf\n\t"
    "v_mov_b32_dpp v40, v32 quad_perm:[0,2,0,2] row_mask:0xf bank_mask:0xf\n\t"
    "v_mov_b32_dpp v41, v33 quad_perm:[0,2,0,2] row_mask:0xf bank_mask:0xf\n\t"
    "v_mov_b32_dpp v42, v34 quad_perm:[0,2,0,2] row_mask:0xf bank_mask:0xf\n\t"
    "v_mov_b32_dpp v43, v35 quad_perm:[0,2,0,2] row_mask:0xf bank_mask:0xf\n\t"
    "v_perm_b32 v32, v36, v40, v13\n\t"
    "buffer_store_dword v32, v12, s[88:91], 0 offen sc1\n\t"
    "v_perm_b32 v33, v37, v41, v13\n\t"
    "buffer_store_dword v33, v17, s[88:91], 0 offen sc1\n\t"
    "v_perm_b32 v34, v38, v42, v13\n\t"
    "buffer_store_dword v34, v18, s[88:91], 0 offen sc1\n\t"
    "v_perm_b32 v35, v39, v43, v13\n\t"
    "buffer_store_dword v35, v19, s[88:91], 0 offen sc1\n\t"
    "s_cmp_lg_u32 s57, 0\n\t"
    "s_cbranch_scc1 .Lfin_%=\n\t"
    "s_cmp_ge_u32 s22, 12\n\t"
    "s_cbranch_scc1 .Lnonce_%=\n\t"
    "s_mov_b32 s57, 1\n\t"
    "v_add_u32 v11, 8704, v11\n\t"
    "v_add_u32 v12, 0x200000, v12\n\t"
    "s_setprio 3\n\t"
    "s_barrier\n\t"
    "s_branch .Ltile_%=\n\t"
    ".Lfin_%=:\n\t"
    ".Lend_%=:\n\t"
    "s_endpgm\n\t"
    ".Lafin_%=:\n\t"
    "s_waitcnt lgkmcnt(0)\n\t"
    "s_barrier\n\t"
    "s_endpgm\n\t"
    ".Lcont_%=:\n\t"
    "v_add_u32 v1, 192, v1\n\t"
    "s_branch .Lcontb_%=\n\t"
    ".Ltouch5_%=:\n\t"
    "s_branch .Lbw_%=\n\t"
    ".Lins3_%=:\n\t"
    "s_or_b32 s24, vcc_lo, vcc_hi\n\t"
    "s_and_b32 s24, s24, 0x80808080\n\t"
    "s_cbranch_scc1 .Lbail_%=\n\t"
    "s_mov_b64 s[46:47], vcc\n\t"
    "s_mov_b64 exec, vcc\n\t"
    "v_lshlrev_b32_sdwa v75, 2, v67 dst_sel:DWORD dst_unused:UNUSED_PAD src0_sel:DWORD src1_sel:BYTE_0\n\t"
    "ds_add_rtn_u32 v83, v75, v23 offset:1024\n\t"
    "v_and_b32 v67, 0xffffff00, v67\n\t"
    "s_waitcnt lgkmcnt(0)\n\t"
    "s_mov_b64 exec, s[46:47]\n\t"
    "v_min_u32 v83, 0x100, v83\n\t"
    "v_mad_u32_u24 v83, v75, s23, v83\n\t"
    "ds_write_b32 v83, v67 offset:1280\n\t"
    "s_mov_b64 exec, -1\n\t"
    "s_branch .Lins3b_%=\n\t"
    ".Lbail_%=:\n\t"
    "s_waitcnt vmcnt(0)\n\t"
    "s_mov_b64 exec, -1\n\t"
    "v_mov_b32 v45, 0x7fffffff\n\t"
    "ds_write_b32 v8, v45 offset:1276\n\t"
    "s_branch .Lins_done_%=\n\t"
    ".Lackst_%=:\n\t"
    "s_cmp_lg_u32 s22, 15\n\t"
    "s_cbranch_scc1 .Lackstb_%=\n\t"
    "s_cmp_lt_u32 %[b], 128\n\t"
    "s_cbranch_scc1 .Lackstb_%=\n\t"
    "s_lshl_b32 s23, %[b], 4\n\t"
    "v_mov_b32 v17, s23\n\t"
    "v_mov_b64 v[20:21], s[36:37]\n\t"
    "v_mov_b64 v[22:23], s[38:39]\n\t"
    "s_mov_b64 exec, 1\n\t"
    "global_store_dwordx4 v17, v[20:23], s[12:13] offset:0 sc1\n\t"
    "s_mov_b64 exec, -1\n\t"
    "s_branch .Lackstb_%=\n\t"
    ".Lnonce_%=:\n\t"
    "s_barrier\n\t"
    "s_cmp_lg_u32 %[b], 255\n\t"
    "s_cbranch_scc1 .Lend_%=\n\t"
    "s_cmp_lg_u32 s22, 15\n\t"
    "s_cbranch_scc1 .Lend_%=\n\t"
    "v_lshlrev_b32 v16, 2, v14\n\t"
    "s_mov_b32 s27, 0x40000\n\t"
    ".Lacka_%=:\n\t"
    "global_load_dwordx4 v[64:67], v16, s[12:13] offset:2048 sc1\n\t"
    "global_load_dwordx4 v[68:71], v16, s[12:13] offset:3072 sc1\n\t"
    "s_sub_u32 s27, s27, 1\n\t"
    "s_waitcnt vmcnt(0)\n\t"
    "s_cbranch_scc1 .Lackda_%=\n\t"
    "v_xor_b32 v64, s36, v64\n\t"
    "v_xor_b32 v65, s37, v65\n\t"
    "v_xor_b32 v66, s38, v66\n\t"
    "v_xor_b32 v67, s39, v67\n\t"
    "v_xor_b32 v68, s36, v68\n\t"
    "v_xor_b32 v69, s37, v69\n\t"
    "v_xor_b32 v70, s38, v70\n\t"
    "v_xor_b32 v71, s39, v71\n\t"
    "v_or3_b32 v64, v64, v65, v66\n\t"
    "v_or3_b32 v67, v67, v68, v69\n\t"
    "v_or3_b32 v64, v64, v70, v71\n\t"
    "v_or_b32 v64, v64, v67\n\t"
    "v_cmp_ne_u32 vcc, 0, v64\n\t"
    "s_cbranch_vccnz .Lacka_%=\n\t"
    ".Lackda_%=:\n\t"
    "v_mov_b64 v[64:65], s[36:37]\n\t"
    "v_mov_b64 v[66:67], s[38:39]\n\t"
    "s_mov_b64 exec, 1\n\t"
    "global_store_dwordx4 v16, v[64:67], s[12:13] sc1\n\t"
    "s_mov_b64 exec, -1\n\t"
    "s_branch .Lend_%=\n\t"
    ".Lslow_%=:\n\t"
    "s_cmp_lg_u32 %[b], 255\n\t"
    "s_cbranch_scc1 .Lslow1_%=\n\t"
    "s_cmp_lg_u32 s22, 15\n\t"
    "s_cbranch_scc1 .Lslow1_%=\n\t"
    "v_lshlrev_b32 v16, 2, v14\n\t"
    "s_mov_b32 s27, 0x40000\n\t"
    ".Lackb_%=:\n\t"
    "global_load_dwordx4 v[64:67], v16, s[12:13] offset:2048 sc1\n\t"
    "global_load_dwordx4 v[68:71], v16, s[12:13] offset:3072 sc1\n\t"
    "s_sub_u32 s27, s27, 1\n\t"
    "s_waitcnt vmcnt(0)\n\t"
    "s_cbranch_scc1 .Lackdb_%=\n\t"
    "v_xor_b32 v64, s36, v64\n\t"
    "v_xor_b32 v65, s37, v65\n\t"
    "v_xor_b32 v66, s38, v66\n\t"
    "v_xor_b32 v67, s39, v67\n\t"
    "v_xor_b32 v68, s36, v68\n\t"
    "v_xor_b32 v69, s37, v69\n\t"
    "v_xor_b32 v70, s38, v70\n\t"
    "v_xor_b32 v71, s39, v71\n\t"
    "v_or3_b32 v64, v64, v65, v66\n\t"
    "v_or3_b32 v67, v67, v68, v69\n\t"
    "v_or3_b32 v64, v64, v70, v71\n\t"
    "v_or_b32 v64, v64, v67\n\t"
    "v_cmp_ne_u32 vcc, 0, v64\n\t"
    "s_cbranch_vccnz .Lackb_%=\n\t"
    ".Lackdb_%=:\n\t"
    "v_mov_b64 v[64:65], s[36:37]\n\t"
    "v_mov_b64 v[66:67], s[38:39]\n\t"
    "s_mov_b64 exec, 1\n\t"
    "global_store_dwordx4 v16, v[64:67], s[12:13] sc1\n\t"
    "s_mov_b64 exec, -1\n\t"
    ".Lslow1_%=:\n\t"
    "s_setprio 0\n\t"
    "s_waitcnt vmcnt(0) lgkmcnt(0)\n\t"
    :
    : [ka] "s"(__builtin_amdgcn_kernarg_segment_ptr()), [b] "s"(wg), [tid] "v"(tid), [cepp] "s"(500)
    : "memory", "scc", "vcc", "v1", "v2", "v3", "v4", "v5", "v6", "v7", "v8", "v9", "v10", "v11", "v12", "v13", "v14", "v15", "v16", "v17", "v18", "v19", "v20", "v21", "v22", "v23", "v24", "v25", "v26", "v27", "v28", "v29", "v30", "v31", "v32", "v33", "v34", "v35", "v36", "v37", "v38", "v39", "v40", "v41", "v42", "v43", "v44", "v45", "v46", "v47", "v48", "v49", "v50", "v51", "v52", "v53", "v54", "v55", "v56", "v57", "v58", "v59", "v60", "v61", "v62", "v63", "v64", "v65", "v66", "v67", "v68", "v69", "v70", "v71", "v72", "v73", "v74", "v75", "v76", "v77", "v78", "v79", "v80", "v81", "v82", "v83", "v84", "v85", "v86", "v87", "v88", "v89", "v90", "v91", "v92", "v93", "v94", "v95", "v96", "v97", "v98", "v99", "v100", "v101", "v102", "v103", "v104", "v105", "v106", "v107", "v108", "v109", "v110", "v111", "v112", "v113", "v114", "v115", "v116", "v117", "v118", "v119", "v120", "v121", "v122", "v123", "v124", "v125", "v126", "v127", "s4", "s5", "s6", "s7", "s8", "s9", "s10", "s11", "s12", "s13", "s14", "s15", "s16", "s17", "s18", "s19", "s20", "s21", "s22", "s23", "s24", "s25", "s26", "s27", "s28", "s29", "s30", "s31", "s33", "s34", "s35", "s36", "s37", "s38", "s39", "s40", "s41", "s42", "s43", "s44", "s45", "s46", "s47", "s48", "s49", "s50", "s51", "s52", "s53", "s54", "s55", "s56", "s57", "s58", "s59", "s60", "s61", "s62", "s63", "s64", "s65", "s66", "s67", "s68", "s69", "s70", "s71", "s72", "s73", "s74", "s75", "s76", "s77", "s78", "s79", "s80", "s81", "s82", "s83", "s84", "s85", "s86", "s87", "s88", "s89", "s90", "s91", "s92", "s93", "s94", "s95", "s96", "s97", "s98", "s99");
  const u32 lb = (u32)(size_t)(LAS char*)smem, b = (wg & ~7u) | ((wg + (wg >> 3)) & 7u);
  const u32 lane = tid & 63, w = __builtin_amdgcn_readfirstlane(tid >> 6), nn = (NN - b + 255) >> 8;
  constexpr float KS = -0.08838834764831845f * 1.4426950408889634f;
  for (u32 i = tid; i < 40 * 128; i += 1024) LDS32(lb + L_STAGE + i * 4) = 0;
  __syncthreads();
  for (u32 e0 = w * 64; e0 < EE; e0 += 1024) {
    const u32 e = e0 + lane;
    const u32 d = e < EE ? dst[e] : ~b, s = e < EE ? src[e] : 0;
    unsigned long long m = __ballot((d & 255) == b);
    while (m) {
      const int j = __builtin_ctzll(m); m &= m - 1;
      const u32 sj = __builtin_amdgcn_readlane(s, j), dl = __builtin_amdgcn_readlane(d, j) >> 8;
      const u32 xv = ((const u32*)x)[sj * 64 + lane], qv = LDS32(lb + L_Q + dl * RSTR + lane * 4);
      const float x0 = __uint_as_float(xv << 16), x1 = __uint_as_float(xv & 0xFFFF0000u);
      float pp = x0 * __uint_as_float(qv << 16) + x1 * __uint_as_float(qv & 0xFFFF0000u);
#pragma unroll
      for (int o = 32; o; o >>= 1) pp += __shfl_xor(pp, o);
      float al = __builtin_amdgcn_rcpf(1.0f + __builtin_amdgcn_exp2f(pp * KS));
      al = __uint_as_float(pk_bf16(al, al) << 16);
      __hip_atomic_fetch_add((LAS float*)(lb + L_STAGE + (dl * 128 + 2 * lane) * 4), al * x0, __ATOMIC_RELAXED, __HIP_MEMORY_SCOPE_WORKGROUP);
      __hip_atomic_fetch_add((LAS float*)(lb + L_STAGE + (dl * 128 + 2 * lane + 1) * 4), al * x1, __ATOMIC_RELAXED, __HIP_MEMORY_SCOPE_WORKGROUP);
    }
  }
  __syncthreads();
  for (u32 i = tid; i < nn * 128; i += 1024) {
    const u32 dl = i >> 7, n = i & 127;
    float acc = 0.f;
#pragma unroll 4
    for (u32 k = 0; k < 128; ++k) acc += __uint_as_float(LDS32(lb + L_STAGE + (dl * 128 + k) * 4)) * __uint_as_float((u32)wv[k * 128 + n] << 16);
    out[(b + 256 * dl) * 128 + n] = (uint16_t)pk_bf16(acc, acc);
  }
}

extern "C" void kernel_launch(void* const* d_in, const int* in_sizes, int n_in, void* d_out, int out_size, void* d_ws, size_t ws_size,
                              hipStream_t stream) {
  fg_fused<<<256, 1024, 0, stream>>>((const int*)d_in[2], (const int*)d_in[1], (char*)d_ws, (const uint16_t*)d_in[0], (const uint16_t*)d_in[3],
                                     (const uint16_t*)d_in[4], (uint16_t*)d_out);
}
```
